```python
import torch

def _softmax(x, axis=-1):
    return torch.softmax(x, dim=axis)
def _gelu(x):
    return torch.nn.functional.gelu(x, approximate="tanh")
def _silu(x):
    return torch.nn.functional.silu(x)
def _sigmoid(x):
    return torch.sigmoid(x)
def _layernorm(x, axis=-1, eps=1e-5):
    m = x.mean(dim=axis, keepdim=True)
    v = x.var(dim=axis, keepdim=True, unbiased=False)
    return (x - m) / torch.sqrt(v + eps)
def _scatter_add(tgt, idx, src, axis=0):
    return tgt.clone().index_add_(axis, idx, src)
def _segment_sum(src, seg, n):
    out = torch.zeros((n,) + src.shape[1:], dtype=src.dtype)
    return out.index_add_(0, seg, src)
def _pad(x, pw):
    flat = tuple(v for pair in reversed(pw) for v in pair)
    return torch.nn.functional.pad(x, flat)

kernel_name = 'parallel_sum__sliding_local_dilated__glu_gated_geglu__prompt'

def setup_inputs():

    _w = (lambda a: (lambda t: t.to(torch.bfloat16) if t.is_floating_point() else t)(torch.as_tensor(a)))
    def _A_setup():
        B, S, D = 1, 2048, 640
        return {
            "x": _w((lambda *s: torch.randn(*s, dtype=torch.bfloat16) * 0.05)(B, S, D)),
            "wqkv": _w((lambda *s: torch.randn(*s, dtype=torch.bfloat16) * 0.05)(D, 3 * D)),
            "wo": _w((lambda *s: torch.randn(*s, dtype=torch.bfloat16) * 0.05)(D, D)),
        }
    _A_in = _A_setup()

    def _B_setup():
        B, S, Dm, Df = 1, 2048, 640, 1728
        return {
            "x": _w((lambda *s: torch.randn(*s, dtype=torch.bfloat16) * 0.05)(B, S, Dm)),
            "w_gate": _w((lambda *s: torch.randn(*s, dtype=torch.bfloat16) * 0.8)(Dm, Df) * (Dm ** -0.5)),
            "w_up": _w((lambda *s: torch.randn(*s, dtype=torch.bfloat16) * 0.8)(Dm, Df) * (Dm ** -0.5)),
            "w_down": _w((lambda *s: torch.randn(*s, dtype=torch.bfloat16) * 0.8)(Df, Dm) * (Df ** -0.5)),
        }
    _B_in = _B_setup()

    return {
        "A_x": _A_in["x"],
        "A_wqkv": _A_in["wqkv"],
        "A_wo": _A_in["wo"],
        "B_x": _B_in["x"],
        "B_w_gate": _B_in["w_gate"],
        "B_w_up": _B_in["w_up"],
        "B_w_down": _B_in["w_down"],
    }

def reference(A_x, A_wqkv, A_wo, B_x, B_w_gate, B_w_up, B_w_down):
    def _A_ref(x, wqkv, wo):
        B, S, D = x.shape
        H = 32; Dh = D // H
        W, dil = 8, 2
        qkv = (lambda x, *d: x.permute(*d))((x @ wqkv).reshape(B, S, 3, H, Dh), 2, 0, 3, 1, 4)
        q, k, v = qkv[0], qkv[1], qkv[2]
        scale = Dh ** -0.5
        pos = torch.arange(S)
        off = torch.arange(W) * dil
        idx = pos[:, None] - off[None, :]
        mask = idx >= 0
        idx_c = torch.where(mask, idx, 0).reshape(-1)
        kg = k[:, :, idx_c, :].reshape(B, H, S, W, Dh)
        vg = v[:, :, idx_c, :].reshape(B, H, S, W, Dh)
        logits = torch.einsum("bhsd,bhswd->bhsw", q, kg) * scale
        logits = torch.where(mask[None, None, :, :], logits, -1e9)
        attn = _softmax(logits, axis=-1)
        o = torch.einsum("bhsw,bhswd->bhsd", attn, vg)
        return (lambda x, *d: x.permute(*d))(o, 0, 2, 1, 3).reshape(B, S, D) @ wo

    def _B_ref(x, w_gate, w_up, w_down):
        g = torch.einsum("bsd,df->bsf", x, w_gate)
        u = torch.einsum("bsd,df->bsf", x, w_up)
        h = _gelu(g) * u
        return torch.einsum("bsf,fd->bsd", h, w_down)

    x = A_x
    xn = _layernorm(x, axis=-1)
    return x + _A_ref(xn, A_wqkv, A_wo) + _B_ref(xn, B_w_gate, B_w_up, B_w_down)
```

```cpp
#include <hip/hip_runtime.h>
#include <hip/hip_bf16.h>
typedef __hip_bfloat16 bf16;
static __device__ __forceinline__ float b2f(bf16 v){ return __bfloat162float(v);}
static __device__ __forceinline__ bf16 f2b(float v){ return __float2bfloat16(v);}
constexpr int S=2048, D=640, H=32, DH=20, DF=1728;

__global__ void ln_k(const bf16* x, bf16* xn){
  int r=blockIdx.x; int t=threadIdx.x;
  float s=0; for(int i=t;i<D;i+=64) s+=b2f(x[r*D+i]);
  for(int o=32;o;o>>=1) s+=__shfl_xor(s,o);
  float m=s/D; float v=0; for(int i=t;i<D;i+=64){float d=b2f(x[r*D+i])-m; v+=d*d;}
  for(int o=32;o;o>>=1) v+=__shfl_xor(v,o);
  float rs=rsqrtf(v/D+1e-5f);
  for(int i=t;i<D;i+=64) xn[r*D+i]=f2b((b2f(x[r*D+i])-m)*rs);
}
__global__ void gemm_k(const bf16* A,int lda,const bf16* B,int ldb,float* C,int ldc,int K,int accum){
  __shared__ float As[32][33], Bs[32][33];
  int tx=threadIdx.x, ty=threadIdx.y; int r=blockIdx.y*32+ty, c=blockIdx.x*32+tx; float acc=0;
  for(int k0=0;k0<K;k0+=32){
    As[ty][tx]=b2f(A[(size_t)r*lda+k0+tx]); Bs[ty][tx]=b2f(B[(size_t)(k0+ty)*ldb+c]);
    __syncthreads();
    for(int k=0;k<32;k++) acc+=As[ty][k]*Bs[k][tx];
    __syncthreads();
  }
  if(accum) C[(size_t)r*ldc+c]+=acc; else C[(size_t)r*ldc+c]=acc;
}
__global__ void attn_k(const float* qkv, bf16* o){
  int idx=blockIdx.x*blockDim.x+threadIdx.x; int s=idx/H, h=idx%H;
  float lg[8]; float mx=-1e30f;
  for(int w=0;w<8;w++){ int t=s-2*w; float a=-1e9f; if(t>=0){ a=0; for(int d=0;d<DH;d++) a+=b2f(f2b(qkv[(size_t)s*1920+h*DH+d]))*b2f(f2b(qkv[(size_t)t*1920+640+h*DH+d])); a*=0.22360679775f;} lg[w]=a; mx=fmaxf(mx,a);}
  float sum=0; for(int w=0;w<8;w++){ lg[w]=__expf(lg[w]-mx); sum+=lg[w]; }
  for(int d=0;d<DH;d++){ float a=0; for(int w=0;w<8;w++){ int t=s-2*w; if(t>=0) a+=lg[w]/sum*b2f(f2b(qkv[(size_t)t*1920+1280+h*DH+d])); } o[(size_t)s*D+h*DH+d]=f2b(a);}
}
__global__ void glu_k(const float* g,const float* u,bf16* h,int n){
  int i=blockIdx.x*blockDim.x+threadIdx.x; if(i>=n) return; float x=g[i];
  float t=tanhf(0.7978845608f*(x+0.044715f*x*x*x)); h[i]=f2b(0.5f*x*(1.f+t)*u[i]);
}
__global__ void fin_k(const bf16* x,const float* c,bf16* out,int n){ int i=blockIdx.x*blockDim.x+threadIdx.x; if(i<n) out[i]=f2b(b2f(x[i])+c[i]); }

extern "C" void kernel_launch(void* const* d_in, const int* in_sizes, int n_in, void* d_out, int out_size, void* d_ws, size_t ws_size, hipStream_t stream) {
  const bf16* x=(const bf16*)d_in[0]; const bf16* wqkv=(const bf16*)d_in[1]; const bf16* wo=(const bf16*)d_in[2];
  const bf16* wg=(const bf16*)d_in[4]; const bf16* wu=(const bf16*)d_in[5]; const bf16* wd=(const bf16*)d_in[6];
  char* ws=(char*)d_ws; bf16* xn=(bf16*)ws; ws+=S*D*2; float* qkv=(float*)ws; ws+=(size_t)S*1920*4; float* g=(float*)ws; ws+=(size_t)S*DF*4; float* u=(float*)ws; ws+=(size_t)S*DF*4;
  bf16* o=(bf16*)ws; ws+=S*D*2; bf16* h=(bf16*)ws; ws+=(size_t)S*DF*2; float* c=(float*)ws;
  ln_k<<<S,64,0,stream>>>(x,xn);
  dim3 b(32,32);
  gemm_k<<<dim3(1920/32,S/32),b,0,stream>>>(xn,D,wqkv,1920,qkv,1920,D,0);
  gemm_k<<<dim3(DF/32,S/32),b,0,stream>>>(xn,D,wg,DF,g,DF,D,0);
  gemm_k<<<dim3(DF/32,S/32),b,0,stream>>>(xn,D,wu,DF,u,DF,D,0);
  attn_k<<<S*H/256,256,0,stream>>>(qkv,o);
  glu_k<<<(S*DF+255)/256,256,0,stream>>>(g,u,h,S*DF);
  gemm_k<<<dim3(D/32,S/32),b,0,stream>>>(o,D,wo,D,c,D,D,0);
  gemm_k<<<dim3(D/32,S/32),b,0,stream>>>(h,DF,wd,D,c,D,DF,1);
  fin_k<<<(S*D+255)/256,256,0,stream>>>(x,c,(bf16*)d_out,S*D);
}
```

```cpp
#include <hip/hip_runtime.h>
#include <stdint.h>

typedef __bf16 bf16x2 __attribute__((ext_vector_type(2)));
typedef __bf16 bf16x4 __attribute__((ext_vector_type(4)));
typedef __bf16 bf16x8 __attribute__((ext_vector_type(8)));
typedef float f32x2 __attribute__((ext_vector_type(2)));
typedef float f32x4 __attribute__((ext_vector_type(4)));
typedef unsigned u32x2 __attribute__((ext_vector_type(2)));
typedef unsigned u32x4 __attribute__((ext_vector_type(4)));
typedef int i32x4 __attribute__((ext_vector_type(4)));
#define LDS_AS __attribute__((address_space(3)))
#define DI __device__ __forceinline__

constexpr int S_ = 2048, D_ = 640, DF_ = 1728, NKF = 74;
constexpr unsigned AFR_BYTES = 128u * NKF * 1024u;
constexpr unsigned W2_OFF = 16u << 20;
constexpr unsigned W2_BYTES = 8u * NKF * 5u * 1024u;
constexpr int WSLOT = 16384, NW = 5, XSLOT = 24576, NX = 3;
constexpr int L_W = 0, L_X = NW * WSLOT, L_ST = L_X + NX * XSLOT;
constexpr int L_CS = L_ST + 192 * 8;
constexpr int LDS1 = L_CS + 1024;
constexpr int N_MLP = 154, N_ATT = 96;

DI i32x4 mk_srd(const void* p, unsigned bytes) {
  uint64_t a = (uint64_t)p;
  i32x4 r;
  r[0] = (int)(unsigned)a; r[1] = (int)(unsigned)(a >> 32); r[2] = (int)bytes; r[3] = 0x00020000;
  return r;
}
DI void dma16(i32x4 srd, unsigned voff, unsigned soff, unsigned lds) {
  asm volatile("s_mov_b32 m0, %3\n\ts_nop 0\n\tbuffer_load_dwordx4 %0, %1, %2 offen lds"
               :: "v"(voff), "s"(srd), "s"(soff), "s"(lds) : "memory");
}
#define WAIT_V(n) asm volatile("s_waitcnt vmcnt(" #n ")" ::: "memory")
#define WAIT_L0() asm volatile("s_waitcnt lgkmcnt(0)" ::: "memory")
#define BAR() do { __builtin_amdgcn_s_barrier(); asm volatile("" ::: "memory"); } while (0)
template <int N> DI void wait_v() { asm volatile("s_waitcnt vmcnt(%0)" :: "i"(N) : "memory"); }

DI bf16x8 lds_b128(unsigned a) { return *(LDS_AS bf16x8*)(a); }
DI f32x4 lds_f4(unsigned a) { return *(LDS_AS f32x4*)(a); }
DI bf16x8 lds_tr(unsigned a, int d) {
  bf16x4 lo = __builtin_amdgcn_ds_read_tr16_b64_v4bf16((LDS_AS bf16x4*)(a));
  bf16x4 hi = __builtin_amdgcn_ds_read_tr16_b64_v4bf16((LDS_AS bf16x4*)(a + d));
  return __builtin_shufflevector(lo, hi, 0, 1, 2, 3, 4, 5, 6, 7);
}
DI f32x4 mfma(bf16x8 a, bf16x8 b, f32x4 c) { return __builtin_amdgcn_mfma_f32_16x16x32_bf16(a, b, c, 0, 0, 0); }
DI unsigned pk(float a, float b) { f32x2 f = {a, b}; return __builtin_bit_cast(unsigned, __builtin_convertvector(f, bf16x2)); }
DI bf16x8 mk8(unsigned a, unsigned b, unsigned c, unsigned d) { u32x4 v = {a, b, c, d}; return __builtin_bit_cast(bf16x8, v); }
DI float dot2(unsigned a, unsigned b, float c) {
  return __builtin_amdgcn_fdot2_f32_bf16(__builtin_bit_cast(bf16x2, a), __builtin_bit_cast(bf16x2, b), c, false);
}
DI float x16_add(float v) { u32x2 r = __builtin_amdgcn_permlane16_swap(__float_as_uint(v), __float_as_uint(v), false, false); return __uint_as_float(r[0]) + __uint_as_float(r[1]); }
DI float x32_add(float v) { u32x2 r = __builtin_amdgcn_permlane32_swap(__float_as_uint(v), __float_as_uint(v), false, false); return __uint_as_float(r[0]) + __uint_as_float(r[1]); }
DI float x16_max(float v) { u32x2 r = __builtin_amdgcn_permlane16_swap(__float_as_uint(v), __float_as_uint(v), false, false); return fmaxf(__uint_as_float(r[0]), __uint_as_float(r[1])); }
DI float x32_max(float v) { u32x2 r = __builtin_amdgcn_permlane32_swap(__float_as_uint(v), __float_as_uint(v), false, false); return fmaxf(__uint_as_float(r[0]), __uint_as_float(r[1])); }
constexpr unsigned ONES = 0x3f803f80u;

struct P {
  const void *x, *wqkv, *wo, *wg, *wu, *wd;
  void* out;
  char* ws;
};

template <bool ATT>
DI void phase1(const P& p, int job, int wave, int lane, bool pretile, int pt_unit) {
  constexpr int NT = ATT ? 3 : 6, NC = ATT ? 8 : 4;
  const int g = lane >> 4, r16 = lane & 15, q4 = r16 >> 2, p4 = lane & 3;
  int tile0, tlo, thi, slice = 0, G = 0;
  if (ATT) { G = job & 7; int pp = job >> 3, par = pp / 6; pp -= par * 6; tile0 = par * 64 + 11 * pp - 1; tlo = par * 64; thi = par * 64 + 63; }
  else { int pn = job / 14; slice = job - pn * 14; tile0 = 12 * pn; tlo = 0; thi = 127; }
  const int tg = ATT ? (wave >> 1) : (wave >> 2), cg = ATT ? (wave & 1) : (wave & 3);

  const i32x4 sx = mk_srd(p.x, S_ * D_ * 2);
  unsigned xo[3];
#pragma unroll
  for (int i = 0; i < 3; ++i) {
    int row = (wave * 3 + i) * 8 + (lane >> 3);
    int tt = min(max(tile0 + (row >> 4), tlo), thi);
    int tok = 2 * ((tt & 63) * 16 + (row & 15)) + (tt >> 6);
    xo[i] = tok * (D_ * 2) + (((lane & 7) ^ ((row >> 1) & 7)) << 4);
  }
  i32x4 sw; unsigned wo_[2]; unsigned wstep;
  if (ATT) {
    sw = mk_srd(p.wqkv, D_ * 1920 * 2); wstep = 32 * 3840;
#pragma unroll
    for (int i = 0; i < 2; ++i) {
      int idx = min((wave * 2 + i) * 64 + lane, 959);
      int m = idx / 320, rem = idx - m * 320, rho = rem / 10, c = rem - rho * 10;
      int k = (rho & 19) | ((rho & 4) << 1) | ((rho & 8) >> 1);
      wo_[i] = k * 3840 + m * 1280 + G * 160 + c * 16;
    }
  } else {
    sw = mk_srd((wave & 4) ? p.wu : p.wg, D_ * DF_ * 2); wstep = 32 * 3456;
    int lc = (wave >> 1) & 1; if (slice == 13) lc = 0;
#pragma unroll
    for (int i = 0; i < 2; ++i) {
      int row = ((wave & 1) * 2 + i) * 8 + (lane >> 3);
      int f = ((row >> 1) & 1) | (((row >> 3) & 1) << 1);
      wo_[i] = row * 3456 + slice * 256 + lc * 128 + (((lane & 7) ^ (f << 1)) << 4);
    }
  }
  const unsigned wdst = L_W + wave * 2048, xdst = L_X + wave * 3072;
  auto issueW = [&](int step, int slot_off) {
    dma16(sw, wo_[0], step * wstep, wdst + slot_off);
    dma16(sw, wo_[1], step * wstep, wdst + slot_off + 1024);
  };
  const bool skip0 = ATT && wave == 0 && lane < 8;
  auto issueX = [&](int c, int slot_off) {
    if (!skip0) dma16(sx, xo[0], c * 128, xdst + slot_off);
    dma16(sx, xo[1], c * 128, xdst + slot_off + 1024);
    dma16(sx, xo[2], c * 128, xdst + slot_off + 2048);
  };

  const unsigned ptl = L_W + 4 * WSLOT + wave * 5120;
  if (pretile) {
    int kf = pt_unit >> 3, ct = pt_unit & 7;
    const i32x4 s2 = kf < 54 ? mk_srd(p.wd, DF_ * D_ * 2) : mk_srd(p.wo, D_ * D_ * 2);
#pragma unroll
    for (int i = 0; i < 5; ++i) {
      int idx = i * 64 + lane, rho = idx / 10, c = idx - rho * 10;
      int h = rho >> 4, gg = (rho >> 2) & 3, j = rho & 3, row;
      if (kf < 54) row = 32 * kf + rho;
      else if (kf < 70) row = (2 * (kf - 54) + h) * 20 + 4 * gg + j;
      else row = (8 * (kf - 70) + 4 * h + gg) * 20 + 16 + j;
      dma16(s2, row * 1280 + ct * 160 + c * 16, 0, ptl + i * 1024);
    }
  }
  if (ATT && wave == 0 && lane < 24) {
    u32x4 o = {ONES, ONES, ONES, ONES};
    *(LDS_AS u32x4*)(L_X + (lane >> 3) * XSLOT + (lane & 7) * 16) = o;
  }
  issueW(0, 0); issueX(0, 0); issueW(1, WSLOT); issueW(2, 2 * WSLOT); issueX(1, XSLOT); issueW(3, 3 * WSLOT); issueX(2, 2 * XSLOT);
  if (pretile) {
    WAIT_V(17);
    int kf = pt_unit >> 3, ct = pt_unit & 7;
    u32x4* dst = (u32x4*)(p.ws + W2_OFF + (unsigned)((ct * NKF + kf) * 5) * 1024u) + lane;
    unsigned a = ptl + (4 * g + q4) * 160;
#pragma unroll
    for (int nt = 0; nt < 5; ++nt) {
      int cb = nt < 4 ? (nt >> 1) * 64 + p4 * 16 + (nt & 1) * 8 : 128 + p4 * 8;
      bf16x8 v = lds_tr(a + cb, 16 * 160);
      dst[nt * 64] = __builtin_bit_cast(u32x4, v);
    }
  }

  unsigned xb[2], wb[3], sb;
  xb[0] = L_X + (tg * NT * 16 + r16) * 128 + ((g ^ (r16 >> 1)) << 4);
  xb[1] = xb[0] ^ 64;
  sb = xdst + lane * 16;
  if (ATT) {
    unsigned rb = L_W + ((g >> 1) * 16 + (g & 1) * 4 + q4) * 160;
    wb[0] = rb + cg * 80 + p4 * 8;
    wb[1] = rb + cg * 80 + (p4 >> 1) * 5120 + (p4 & 1) * 40 + 32;
    wb[2] = rb + 10240 + p4 * 40 + 32;
  } else {
    int row = 8 * g + q4, f = ((row >> 1) & 1) | (((row >> 3) & 1) << 1);
    int uA = (2 * (cg & 1)) | tg;
    unsigned rb = L_W + (cg >> 1) * 4096 + row * 128 + p4 * 8;
    wb[0] = rb + ((uA ^ f) << 5);
    wb[1] = rb + ((uA ^ 1 ^ f) << 5);
    wb[2] = 0;
  }

  f32x4 acc[NT][NC];
#pragma unroll
  for (int t = 0; t < NT; ++t)
#pragma unroll
    for (int c = 0; c < NC; ++c) acc[t][c] = f32x4{0, 0, 0, 0};
  bf16x8 xf[2][NT], wf[2][NC];
  u32x4 st[3];
  float ssum[3] = {0, 0, 0}, ssq[3] = {0, 0, 0}, cs0 = 0, cs1 = 0;

  auto rd = [&](int b, int e, unsigned wofs, unsigned xofs) {
    if (ATT) {
      unsigned a = wb[0] + wofs;
      wf[b][0] = lds_tr(a, 1280); wf[b][1] = lds_tr(a + 40, 1280);
      wf[b][2] = lds_tr(a + 5120, 1280); wf[b][3] = lds_tr(a + 5160, 1280);
      wf[b][4] = lds_tr(wb[1] + wofs, 1280);
      wf[b][5] = lds_tr(a + 10240, 1280); wf[b][6] = lds_tr(a + 10280, 1280);
      wf[b][7] = lds_tr(wb[2] + wofs, 1280);
    } else {
      unsigned a = wb[0] + wofs, a2 = wb[1] + wofs;
      wf[b][0] = lds_tr(a, 512); wf[b][1] = lds_tr(a + 8192, 512);
      wf[b][2] = lds_tr(a2, 512); wf[b][3] = lds_tr(a2 + 8192, 512);
    }
    unsigned xa = xb[e] + xofs;
#pragma unroll
    for (int t = 0; t < NT; ++t) xf[b][t] = lds_b128(xa + t * 2048);
    if (e == 0) {
#pragma unroll
      for (int i = 0; i < 3; ++i) st[i] = *(LDS_AS u32x4*)(sb + xofs + i * 1024);
    }
  };
  auto mm = [&](int b, int e) {
#pragma unroll
    for (int t = 0; t < NT; ++t)
#pragma unroll
      for (int c = 0; c < NC; ++c)
        acc[t][c] = (ATT && c >= 5) ? mfma(xf[b][t], wf[b][c], acc[t][c]) : mfma(wf[b][c], xf[b][t], acc[t][c]);
    if (!ATT) {
      u32x4 a = __builtin_bit_cast(u32x4, wf[b][0]), c = __builtin_bit_cast(u32x4, wf[b][1]);
#pragma unroll
      for (int i = 0; i < 4; ++i) { cs0 = dot2(a[i], ONES, cs0); cs1 = dot2(c[i], ONES, cs1); }
    }
    if (e == 0) {
#pragma unroll
      for (int i = 0; i < 3; ++i)
#pragma unroll
        for (int j = 0; j < 4; ++j) { ssum[i] = dot2(st[i][j], ONES, ssum[i]); ssq[i] = dot2(st[i][j], st[i][j], ssq[i]); }
    }
  };

  WAIT_V(12); WAIT_L0(); BAR();
  issueW(4, 4 * WSLOT);
  rd(0, 0, 0, 0);
  int ws_ = 0, xs_ = 0;
  auto nextW = [&](int o) { o += WSLOT; return o == NW * WSLOT ? 0 : o; };
  auto nextX = [&](int o) { o += XSLOT; return o == NX * XSLOT ? 0 : o; };
  for (int c = 0; c < 7; ++c) {
    int w1 = nextW(ws_), w2 = nextW(w1), x1 = nextX(xs_);
    WAIT_V(12); WAIT_L0(); BAR();
    issueW(2 * c + 5, ws_);
    rd(1, 1, w1, xs_);
    mm(0, 0);
    WAIT_V(9); WAIT_L0(); BAR();
    issueW(2 * c + 6, w1); issueX(c + 3, xs_);
    rd(0, 0, w2, x1);
    mm(1, 1);
    ws_ = w2; xs_ = x1;
  }
  WAIT_V(12); WAIT_L0(); BAR();
  issueW(19, 4 * WSLOT);
  rd(1, 1, 0 * WSLOT, 1 * XSLOT); mm(0, 0);
  WAIT_V(9); WAIT_L0(); BAR();
  rd(0, 0, 1 * WSLOT, 2 * XSLOT); mm(1, 1);
  WAIT_V(7); WAIT_L0(); BAR();
  rd(1, 1, 2 * WSLOT, 2 * XSLOT); mm(0, 0);
  WAIT_V(2); WAIT_L0(); BAR();
  rd(0, 0, 3 * WSLOT, 0 * XSLOT); mm(1, 1);
  WAIT_V(0); WAIT_L0(); BAR();
  rd(1, 1, 4 * WSLOT, 0 * XSLOT); mm(0, 0);
  mm(1, 1);

#pragma unroll
  for (int i = 0; i < 3; ++i) {
    float s = ssum[i], q = ssq[i];
    s += __shfl_xor(s, 1); q += __shfl_xor(q, 1);
    s += __shfl_xor(s, 2); q += __shfl_xor(q, 2);
    s += __shfl_xor(s, 4); q += __shfl_xor(q, 4);
    float mu = s * (1.0f / D_);
    float var = fmaxf(q * (1.0f / D_) - mu * mu, 0.0f);
    float rs = __builtin_amdgcn_rsqf(var + 1e-5f);
    if ((lane & 7) == 0) *(LDS_AS f32x2*)(L_ST + ((wave * 3 + i) * 8 + (lane >> 3)) * 8) = f32x2{rs, -rs * mu};
  }
  u32x4* afr = (u32x4*)p.ws;
  if (!ATT) {
    cs0 = x32_add(x16_add(cs0)); cs1 = x32_add(x16_add(cs1));
    if (lane < 16) {
      int ft = cg * 2 + tg;
      *(LDS_AS float*)(L_CS + (ft * 16 + lane) * 4) = cs0;
      *(LDS_AS float*)(L_CS + 512 + (ft * 16 + lane) * 4) = cs1;
    }
    WAIT_L0(); BAR();
    f32x4 cs[4];
    cs[0] = lds_f4(L_CS + ((cg * 2 + tg) * 16 + 4 * g) * 4);
    cs[1] = lds_f4(L_CS + 512 + ((cg * 2 + tg) * 16 + 4 * g) * 4);
    cs[2] = lds_f4(L_CS + ((cg * 2 + (tg ^ 1)) * 16 + 4 * g) * 4);
    cs[3] = lds_f4(L_CS + 512 + ((cg * 2 + (tg ^ 1)) * 16 + 4 * g) * 4);
    const int kf = slice * 4 + cg;
#pragma unroll
    for (int t = 0; t < NT; ++t) {
      f32x2 ab = *(LDS_AS f32x2*)(L_ST + ((tg * NT + t) * 16 + r16) * 8);
      float h[2][4];
#pragma unroll
      for (int s2 = 0; s2 < 2; ++s2)
#pragma unroll
        for (int r = 0; r < 4; ++r) {
          float gv = fmaf(ab[1], cs[2 * s2][r], ab[0] * acc[t][2 * s2][r]);
          float uv = fmaf(ab[1], cs[2 * s2 + 1][r], ab[0] * acc[t][2 * s2 + 1][r]);
          float e = __builtin_amdgcn_exp2f(gv * fmaf(gv * gv, -0.10294324f, -2.3022082f));
          h[s2][r] = gv * uv * __builtin_amdgcn_rcpf(1.0f + e);
        }
      unsigned a0 = pk(h[0][0], h[0][1]), a1 = pk(h[0][2], h[0][3]), b0 = pk(h[1][0], h[1][1]), b1 = pk(h[1][2], h[1][3]);
      u32x4 v = tg ? u32x4{b0, b1, a0, a1} : u32x4{a0, a1, b0, b1};
      int tt = tile0 + tg * NT + t;
      if (tt <= 127 && kf < 54) afr[(((tt >> 2) * NKF + kf) * 4 + (tt & 3)) * 64 + lane] = v;
    }
  } else {
    if (tg == 0) {
      if (r16 == 0) {
#pragma unroll
        for (int c = 0; c < 5; ++c) *(LDS_AS f32x4*)(L_CS + ((cg * 8 + c) * 16 + 4 * g) * 4) = acc[0][c];
      }
      if (lane < 16) {
#pragma unroll
        for (int c = 5; c < 8; ++c) *(LDS_AS float*)(L_CS + ((cg * 8 + c) * 16 + lane) * 4) = acc[0][c][0];
      }
    }
    WAIT_L0(); BAR();
    constexpr float QS = 0.22360679775f * 1.44269504089f;
    f32x4 cs[5]; float cv[3];
#pragma unroll
    for (int c = 0; c < 5; ++c) cs[c] = lds_f4(L_CS + ((cg * 8 + c) * 16 + 4 * g) * 4);
#pragma unroll
    for (int c = 0; c < 3; ++c) cv[c] = *(LDS_AS float*)(L_CS + ((cg * 8 + 5 + c) * 16 + r16) * 4);
    unsigned Q[3][2][2], Qt[3][2], K[4][3][2], V[4][3][2];
#pragma unroll
    for (int t = 0; t < 3; ++t) {
      f32x2 ab = *(LDS_AS f32x2*)(L_ST + ((tg * 3 + t) * 16 + r16) * 8);
      f32x4 v0 = lds_f4(L_ST + ((tg * 3 + t) * 16 + 4 * g) * 8), v1 = lds_f4(L_ST + ((tg * 3 + t) * 16 + 4 * g) * 8 + 16);
      float aq = ab[0] * QS, bq = ab[1] * QS;
      float at = g < 2 ? aq : ab[0], bt = g < 2 ? bq : ab[1];
      float y[4];
#pragma unroll
      for (int c = 0; c < 5; ++c) {
        float a = c < 2 ? aq : (c < 4 ? ab[0] : at), b = c < 2 ? bq : (c < 4 ? ab[1] : bt);
#pragma unroll
        for (int r = 0; r < 4; ++r) y[r] = fmaf(b, cs[c][r], a * acc[t][c][r]);
        unsigned lo = pk(y[0], y[1]), hi = pk(y[2], y[3]);
        if (c < 2) { Q[t][c][0] = lo; Q[t][c][1] = hi; }
        else if (c < 4) { K[t + 1][c - 2][0] = lo; K[t + 1][c - 2][1] = hi; }
        else {
          u32x2 s0 = __builtin_amdgcn_permlane32_swap(lo, lo, false, false);
          u32x2 s1 = __builtin_amdgcn_permlane32_swap(hi, hi, false, false);
          Qt[t][0] = s0[0]; K[t + 1][2][0] = s0[1]; Qt[t][1] = s1[0]; K[t + 1][2][1] = s1[1];
        }
      }
#pragma unroll
      for (int c = 0; c < 3; ++c) {
        y[0] = fmaf(v0[1], cv[c], v0[0] * acc[t][5 + c][0]);
        y[1] = fmaf(v0[3], cv[c], v0[2] * acc[t][5 + c][1]);
        y[2] = fmaf(v1[1], cv[c], v1[0] * acc[t][5 + c][2]);
        y[3] = fmaf(v1[3], cv[c], v1[2] * acc[t][5 + c][3]);
        V[t + 1][c][0] = pk(y[0], y[1]); V[t + 1][c][1] = pk(y[2], y[3]);
      }
    }
    {
      unsigned ex = L_W + wave * 3072 + lane * 16;
      *(LDS_AS u32x4*)(ex) = u32x4{K[3][0][0], K[3][0][1], K[3][1][0], K[3][1][1]};
      *(LDS_AS u32x4*)(ex + 1024) = u32x4{K[3][2][0], K[3][2][1], V[3][0][0], V[3][0][1]};
      *(LDS_AS u32x4*)(ex + 2048) = u32x4{V[3][1][0], V[3][1][1], V[3][2][0], V[3][2][1]};
      WAIT_L0(); BAR();
      unsigned ey = L_W + ((wave + 6) & 7) * 3072 + lane * 16;
      u32x4 a = *(LDS_AS u32x4*)(ey), b = *(LDS_AS u32x4*)(ey + 1024), c = *(LDS_AS u32x4*)(ey + 2048);
      K[0][0][0] = a[0]; K[0][0][1] = a[1]; K[0][1][0] = a[2]; K[0][1][1] = a[3];
      K[0][2][0] = b[0]; K[0][2][1] = b[1]; V[0][0][0] = b[2]; V[0][0][1] = b[3];
      V[0][1][0] = c[0]; V[0][1][1] = c[1]; V[0][2][0] = c[2]; V[0][2][1] = c[3];
    }
    const f32x4 z4 = {0, 0, 0, 0};
#pragma unroll
    for (int t = 0; t < 3; ++t) {
      const int tt = tile0 + tg * 3 + t;
      const bool has_prev = tt > tlo;
      float om[2][4], ot[2][4];
#pragma unroll
      for (int h = 0; h < 2; ++h) {
        bool mine = g == h;
        bf16x8 qf = mk8(Q[t][h][0], Q[t][h][1], mine ? Qt[t][0] : 0u, mine ? Qt[t][1] : 0u);
        f32x4 sp = mfma(mk8(K[t][h][0], K[t][h][1], K[t][2][0], K[t][2][1]), qf, z4);
        f32x4 sc = mfma(mk8(K[t + 1][h][0], K[t + 1][h][1], K[t + 1][2][0], K[t + 1][2][1]), qf, z4);
        float m = -INFINITY;
#pragma unroll
        for (int r = 0; r < 4; ++r) {
          int kk = 4 * g + r;
          sp[r] = (has_prev && kk >= r16 + 9) ? sp[r] : -INFINITY;
          sc[r] = (kk <= r16 && kk + 7 >= r16) ? sc[r] : -INFINITY;
          m = fmaxf(m, fmaxf(sp[r], sc[r]));
        }
        m = x32_max(x16_max(m));
        float sum = 0;
#pragma unroll
        for (int r = 0; r < 4; ++r) {
          sp[r] = __builtin_amdgcn_exp2f(sp[r] - m); sc[r] = __builtin_amdgcn_exp2f(sc[r] - m);
          sum += sp[r] + sc[r];
        }
        sum = x32_add(x16_add(sum));
        float inv = __builtin_amdgcn_rcpf(sum);
        bf16x8 pf = mk8(pk(sp[0], sp[1]), pk(sp[2], sp[3]), pk(sc[0], sc[1]), pk(sc[2], sc[3]));
        f32x4 o1 = mfma(mk8(V[t][h][0], V[t][h][1], V[t + 1][h][0], V[t + 1][h][1]), pf, z4);
        f32x4 o2 = mfma(mk8(V[t][2][0], V[t][2][1], V[t + 1][2][0], V[t + 1][2][1]), pf, z4);
#pragma unroll
        for (int r = 0; r < 4; ++r) { om[h][r] = o1[r] * inv; ot[h][r] = o2[r] * inv; }
      }
      if (!(tg == 0 && t == 0) && tt <= thi) {
        unsigned base = ((tt >> 2) * NKF) * 4 + (tt & 3);
        afr[(base + (54 + 2 * G + cg) * 4) * 64 + lane] =
            u32x4{pk(om[0][0], om[0][1]), pk(om[0][2], om[0][3]), pk(om[1][0], om[1][1]), pk(om[1][2], om[1][3])};
        if ((g >> 1) == cg) {
          bool b = g & 1;
          u32x2 v = {pk(b ? ot[1][0] : ot[0][0], b ? ot[1][1] : ot[0][1]), pk(b ? ot[1][2] : ot[0][2], b ? ot[1][3] : ot[0][3])};
          ((u32x2*)(afr + (base + (70 + (G >> 1)) * 4) * 64 + lane))[G & 1] = v;
        }
      }
    }
  }
}

__global__ __launch_bounds__(512, 2) void k_phase1(P p) {
  const int lane = threadIdx.x & 63, wave = __builtin_amdgcn_readfirstlane(threadIdx.x >> 6);
  const int b = blockIdx.x;
  const int unit = b * 3 + wave;
  const bool pt = wave < 3 && unit < NKF * 8;
  const int xcd = b & 7, i = b >> 3;
  if (i < 12) {
    int G = (xcd & 3) * 2 + (i & 1), pp = (i >> 1) % 3 + (xcd >> 2) * 3 + ((i >> 1) / 3) * 6;
    phase1<true>(p, pp * 8 + G, wave, lane, pt, unit);
  } else {
    int n = xcd * 20 + (i - 12);
    if (n >= N_MLP) return;
    phase1<false>(p, n, wave, lane, pt, unit);
  }
}

constexpr int LDS2 = 8 * 20 * 1024;
__global__ __launch_bounds__(512, 2) void k_phase2(P p) {
  const int lane = threadIdx.x & 63, wave = __builtin_amdgcn_readfirstlane(threadIdx.x >> 6);
  const int b = blockIdx.x, xcd = b & 7, i = b >> 3;
  const int rt = xcd * 4 + (i >> 3), ct = i & 7;
  const int k0 = wave * 9 + min(wave, 2), nk = wave < 2 ? 10 : 9;
  const u32x4* A = (const u32x4*)p.ws + (unsigned)((rt * NKF + k0) * 4) * 64u + lane;
  const u32x4* W = (const u32x4*)(p.ws + W2_OFF) + (unsigned)((ct * NKF + k0) * 5) * 64u + lane;
  f32x4 acc[4][5];
#pragma unroll
  for (int t = 0; t < 4; ++t)
#pragma unroll
    for (int n = 0; n < 5; ++n) acc[t][n] = f32x4{0, 0, 0, 0};
  constexpr int DEP = 4;
  u32x4 a[DEP][4], w[DEP][5];
  auto ld = [&](int s, int bb) {
#pragma unroll
    for (int n = 0; n < 5; ++n) w[bb][n] = W[(s * 5 + n) * 64];
#pragma unroll
    for (int t = 0; t < 4; ++t) a[bb][t] = A[(s * 4 + t) * 64];
  };
  auto mm = [&](int bb) {
#pragma unroll
    for (int t = 0; t < 4; ++t)
#pragma unroll
      for (int n = 0; n < 5; ++n)
        acc[t][n] = mfma(__builtin_bit_cast(bf16x8, w[bb][n]), __builtin_bit_cast(bf16x8, a[bb][t]), acc[t][n]);
  };
#pragma unroll
  for (int s = 0; s < DEP; ++s) ld(s, s);
#pragma unroll
  for (int s = 0; s < 9; ++s) {
    mm(s % DEP);
    if (s + DEP < 9) ld(s + DEP, s % DEP);
    else if (s + DEP == 9) ld(nk - 1, s % DEP);
  }
  if (nk == 10) mm(9 % DEP);
#pragma unroll
  for (int t = 0; t < 4; ++t)
#pragma unroll
    for (int n = 0; n < 5; ++n) *(LDS_AS f32x4*)(wave * 20480 + (t * 5 + n) * 1024 + lane * 16) = acc[t][n];
  __syncthreads();
  const int g = lane >> 4, r16 = lane & 15;
  {
    const int t = wave >> 1, pr = wave & 1;
    const int tt = rt * 4 + t, tok = 2 * ((tt & 63) * 16 + r16) + (tt >> 6);
    const unsigned eo = tok * D_ + ct * 80 + pr * 32 + g * 8;
    u32x4 xr = *(const u32x4*)((const __bf16*)p.x + eo);
    f32x4 s0 = {0, 0, 0, 0}, s1 = {0, 0, 0, 0};
    unsigned la = (t * 5 + pr * 2) * 1024 + lane * 16;
#pragma unroll
    for (int w8 = 0; w8 < 8; ++w8) { s0 += lds_f4(la + w8 * 20480); s1 += lds_f4(la + w8 * 20480 + 1024); }
    u32x4 o;
    o[0] = pk(s0[0] + __uint_as_float(xr[0] << 16), s0[1] + __uint_as_float(xr[0] & 0xffff0000u));
    o[1] = pk(s0[2] + __uint_as_float(xr[1] << 16), s0[3] + __uint_as_float(xr[1] & 0xffff0000u));
    o[2] = pk(s1[0] + __uint_as_float(xr[2] << 16), s1[1] + __uint_as_float(xr[2] & 0xffff0000u));
    o[3] = pk(s1[2] + __uint_as_float(xr[3] << 16), s1[3] + __uint_as_float(xr[3] & 0xffff0000u));
    *(u32x4*)((__bf16*)p.out + eo) = o;
  }
  if (wave < 4) {
    const int tt = rt * 4 + wave, tok = 2 * ((tt & 63) * 16 + r16) + (tt >> 6);
    const unsigned eo = tok * D_ + ct * 80 + 64 + g * 4;
    u32x2 xr = *(const u32x2*)((const __bf16*)p.x + eo);
    f32x4 s0 = {0, 0, 0, 0};
    unsigned la = (wave * 5 + 4) * 1024 + lane * 16;
#pragma unroll
    for (int w8 = 0; w8 < 8; ++w8) s0 += lds_f4(la + w8 * 20480);
    u32x2 o;
    o[0] = pk(s0[0] + __uint_as_float(xr[0] << 16), s0[1] + __uint_as_float(xr[0] & 0xffff0000u));
    o[1] = pk(s0[2] + __uint_as_float(xr[1] << 16), s0[3] + __uint_as_float(xr[1] & 0xffff0000u));
    *(u32x2*)((__bf16*)p.out + eo) = o;
  }
}

extern "C" void kernel_launch(void* const* d_in, const int* in_sizes, int n_in, void* d_out, int out_size, void* d_ws,
                              size_t ws_size, hipStream_t stream) {
  P p;
  p.x = d_in[0]; p.wqkv = d_in[1]; p.wo = d_in[2]; p.wg = d_in[4]; p.wu = d_in[5]; p.wd = d_in[6];
  p.out = d_out; p.ws = (char*)d_ws;
  (void)hipFuncSetAttribute((const void*)k_phase1, hipFuncAttributeMaxDynamicSharedMemorySize, LDS1);
  (void)hipFuncSetAttribute((const void*)k_phase2, hipFuncAttributeMaxDynamicSharedMemorySize, LDS2);
  k_phase1<<<256, 512, LDS1, stream>>>(p);
  k_phase2<<<256, 512, LDS2, stream>>>(p);
}
```

```cpp
#include <hip/hip_runtime.h>
#include <stdint.h>

typedef __bf16 bf16x2 __attribute__((ext_vector_type(2)));
typedef __bf16 bf16x4 __attribute__((ext_vector_type(4)));
typedef __bf16 bf16x8 __attribute__((ext_vector_type(8)));
typedef float f32x2 __attribute__((ext_vector_type(2)));
typedef float f32x4 __attribute__((ext_vector_type(4)));
typedef unsigned u32x2 __attribute__((ext_vector_type(2)));
typedef unsigned u32x4 __attribute__((ext_vector_type(4)));
typedef unsigned u32x8 __attribute__((ext_vector_type(8)));
typedef int i32x4 __attribute__((ext_vector_type(4)));
#define LDS_AS __attribute__((address_space(3)))
#define DI __device__ __forceinline__
#define PRIOB 1

constexpr int S_ = 2048, D_ = 640, DF_ = 1728, NKF = 74;
constexpr unsigned CTL = 16u << 20;
constexpr unsigned FLG = CTL + 4096;
constexpr unsigned GRN = CTL + 32768;
constexpr unsigned DUM = 24u << 20;
constexpr unsigned WS_BYTES = 32u << 20;
constexpr int WSLOT = 16384, XSLOT = 24576;
constexpr int L_W = 0, L_X = 5 * WSLOT, L_EX = L_X + 2 * XSLOT, L_ST = L_X + 3 * XSLOT, L_CS = L_ST + 2048;
constexpr int LDS_BYTES = 163840;
constexpr int wslot(int s) { return L_W + (s % 5) * WSLOT; }
constexpr int xslot(int c) { return L_X + ((c + 2) % 3) * XSLOT; }
constexpr int SPIN_CAP = 1 << 17;

DI i32x4 mk_srd(const void* p, unsigned bytes) {
  uint64_t a = (uint64_t)p;
  i32x4 r;
  r[0] = (int)(unsigned)a; r[1] = (int)(unsigned)(a >> 32); r[2] = (int)bytes; r[3] = 0x00020000;
  return r;
}
#define PIN(x) asm volatile("" : "+v"(x))
#define SB() __builtin_amdgcn_sched_barrier(0)
#define MB() asm volatile("" ::: "memory")
#define WCNT(v, l) (((v) & 15) | (((v) >> 4) << 14) | 0x70 | ((l) << 8))
#define WAIT_V(n) do { MB(); __builtin_amdgcn_s_waitcnt(WCNT(n, 15)); MB(); } while (0)
#define WAIT_L0() do { MB(); __builtin_amdgcn_s_waitcnt(WCNT(63, 0)); MB(); } while (0)
#define WAIT_VL(n) do { MB(); __builtin_amdgcn_s_waitcnt(WCNT(n, 0)); MB(); } while (0)
#define BAR() do { __builtin_amdgcn_s_barrier(); MB(); } while (0)
#define TS(i)
#define TSR(i)
#define TSDECL
#define TSOUT()

DI bf16x8 lds_b128(unsigned a) { return *(LDS_AS bf16x8*)(a); }
DI f32x4 lds_f4(unsigned a) { return *(LDS_AS f32x4*)(a); }
DI bf16x8 lds_tr(unsigned a, int d) {
  bf16x4 lo = __builtin_amdgcn_ds_read_tr16_b64_v4bf16((LDS_AS bf16x4*)(a));
  bf16x4 hi = __builtin_amdgcn_ds_read_tr16_b64_v4bf16((LDS_AS bf16x4*)(a + d));
  return __builtin_shufflevector(lo, hi, 0, 1, 2, 3, 4, 5, 6, 7);
}
DI f32x4 mfma(bf16x8 a, bf16x8 b, f32x4 c) { return __builtin_amdgcn_mfma_f32_16x16x32_bf16(a, b, c, 0, 0, 0); }
DI unsigned pk(float a, float b) { f32x2 f = {a, b}; return __builtin_bit_cast(unsigned, __builtin_convertvector(f, bf16x2)); }
DI bf16x8 mk8(unsigned a, unsigned b, unsigned c, unsigned d) { u32x4 v = {a, b, c, d}; return __builtin_bit_cast(bf16x8, v); }
DI bf16x8 as8(u32x4 v) { return __builtin_bit_cast(bf16x8, v); }
DI f32x2 sw16(float a, float b) { return __builtin_bit_cast(f32x2, __builtin_amdgcn_permlane16_swap(__float_as_uint(a), __float_as_uint(b), false, false)); }
DI f32x2 sw32(float a, float b) { return __builtin_bit_cast(f32x2, __builtin_amdgcn_permlane32_swap(__float_as_uint(a), __float_as_uint(b), false, false)); }
template <int C> DI float dpp_add(float v) { return v + __uint_as_float(__builtin_amdgcn_update_dpp(0u, __float_as_uint(v), C, 15, 15, true)); }
DI float wave_sum(float v) {
  v = dpp_add<0xB1>(v); v = dpp_add<0x4E>(v); v = dpp_add<0x141>(v); v = dpp_add<0x140>(v);
  f32x2 r = sw16(v, v); v = r[0] + r[1];
  r = sw32(v, v); return r[0] + r[1];
}
DI float blo(unsigned d) { return __uint_as_float(d << 16); }
DI float bhi(unsigned d) { return __uint_as_float(d & 0xffff0000u); }
constexpr unsigned ONES = 0x3f803f80u;

DI unsigned ld1c(i32x4 srd, unsigned vo, unsigned so) {
  unsigned r;
  asm volatile("buffer_load_dword %0, %1, %2, %3 offen sc1" : "=v"(r) : "v"(vo), "s"(srd), "s"(so) : "memory");
  return r;
}
DI u32x4 ld4c(i32x4 srd, unsigned vo, unsigned so) {
  u32x4 r;
  asm volatile("buffer_load_dwordx4 %0, %1, %2, %3 offen sc1" : "=v"(r) : "v"(vo), "s"(srd), "s"(so) : "memory");
  return r;
}
DI void ld16c(u32x4 (&a)[4], i32x4 srd, unsigned vo, unsigned so) {
  unsigned t;
  asm volatile("s_mov_b32 %4, %7\n\tbuffer_load_dwordx4 %0, %5, %6, %4 offen sc1\n\tbuffer_load_dwordx4 %1, %5, %6, %4 offen offset:1024 sc1\n\t"
               "buffer_load_dwordx4 %2, %5, %6, %4 offen offset:2048 sc1\n\tbuffer_load_dwordx4 %3, %5, %6, %4 offen offset:3072 sc1"
               : "=&v"(a[0]), "=&v"(a[1]), "=&v"(a[2]), "=&v"(a[3]), "=&s"(t) : "v"(vo), "s"(srd), "s"(__builtin_amdgcn_readfirstlane(so)) : "memory");
}

DI void st4c(u32x4 d, i32x4 srd, unsigned vo, unsigned so) {
  unsigned t;
  asm volatile("s_mov_b32 %0, %4\n\tbuffer_store_dwordx4 %1, %2, %3, %0 offen sc1\n\ts_nop 1" : "=&s"(t) : "v"(d), "v"(vo), "s"(srd), "s"(__builtin_amdgcn_readfirstlane(so)) : "memory");
}
DI void st4l(u32x4 d, i32x4 srd, unsigned so) {
  unsigned t;
  asm volatile("s_mov_b32 %0, %3\n\ts_mov_b64 exec, 1\n\tbuffer_store_dwordx4 %1, off, %2, %0 sc1\n\ts_mov_b64 exec, -1\n\ts_nop 0"
               : "=&s"(t) : "v"(d), "s"(srd), "s"(__builtin_amdgcn_readfirstlane(so)) : "memory");
}
template <int N, class T> DI void got(T& a) { asm volatile("s_waitcnt vmcnt(%1)" : "+v"(a) : "i"(N) : "memory"); }
template <int N, class T, class U> DI void got(T& a, U& b) { asm volatile("s_waitcnt vmcnt(%2)" : "+v"(a), "+v"(b) : "i"(N) : "memory"); }
template <int N> DI void got4(u32x4 (&a)[4]) {
  asm volatile("s_waitcnt vmcnt(%4)" : "+v"(a[0]), "+v"(a[1]), "+v"(a[2]), "+v"(a[3]) : "i"(N) : "memory");
}
DI int lane_id() {
  int l;
  asm volatile("v_mbcnt_lo_u32_b32 %0, -1, 0\n\tv_mbcnt_hi_u32_b32 %0, -1, %0" : "=v"(l));
  return l;
}
DI unsigned opq(unsigned v) { PIN(v); return v; }
DI unsigned up64k(unsigned v) { unsigned r; asm volatile("v_add_u32 %0, 0x10000, %1" : "=v"(r) : "v"(v)); return r; }

template <int LOFF, int SOFF>
DI void dma2(i32x4 srd, unsigned v0, unsigned v1, unsigned lbase) {
  unsigned t;
  asm volatile("s_add_u32 m0, %4, %5\n\ts_mov_b32 %0, %6\n\tbuffer_load_dwordx4 %1, %3, %0 offen lds\n\t"
               "buffer_load_dwordx4 %2, %3, %0 offen offset:1024 lds"
               : "=&s"(t) : "v"(v0), "v"(v1), "s"(srd), "s"(lbase), "i"(LOFF), "i"(SOFF) : "memory", "scc");
}
template <int LOFF, int SOFF>
DI void dma2m(i32x4 srd, unsigned v0, unsigned v1, unsigned lbase, unsigned long long m) {
  unsigned t;
  asm volatile("s_add_u32 m0, %4, %5\n\ts_mov_b32 %0, %6\n\ts_mov_b64 exec, %7\n\tbuffer_load_dwordx4 %1, %3, %0 offen lds\n\t"
               "s_mov_b64 exec, -1\n\tbuffer_load_dwordx4 %2, %3, %0 offen offset:1024 lds"
               : "=&s"(t) : "v"(v0), "v"(v1), "s"(srd), "s"(lbase), "i"(LOFF), "i"(SOFF), "s"(m) : "memory", "scc");
}
template <int LOFF, int SOFF>
DI void dma1c(i32x4 srd, unsigned v2, unsigned lbase) {
  unsigned t;
  asm volatile("s_add_u32 m0, %3, %4\n\ts_mov_b32 %0, %5\n\tbuffer_load_dwordx4 %1, %2, %0 offen offset:2048 lds"
               : "=&s"(t) : "v"(v2), "s"(srd), "s"(lbase), "i"(LOFF), "i"(SOFF) : "memory", "scc");
}
DI void dma5(i32x4 srd, const unsigned (&v)[5], unsigned soff, unsigned lds) {
  unsigned t;
  asm volatile("s_mov_b32 m0, %8\n\ts_mov_b32 %0, %7\n\tbuffer_load_dwordx4 %1, %6, %0 offen lds\n\t"
               "buffer_load_dwordx4 %2, %6, %0 offen offset:1024 lds\n\t"
               "buffer_load_dwordx4 %3, %6, %0 offen offset:2048 lds\n\t"
               "buffer_load_dwordx4 %4, %6, %0 offen offset:3072 lds\n\t"
               "s_add_u32 m0, %8, 1024\n\ts_nop 0\n\t"
               "buffer_load_dwordx4 %5, %6, %0 offen offset:3072 lds"
               : "=&s"(t) : "v"(v[0]), "v"(v[1]), "v"(v[2]), "v"(v[3]), "v"(v[4]), "s"(srd), "s"(__builtin_amdgcn_readfirstlane(soff)), "s"(lds) : "memory", "scc");
}

struct Op { int kind, idx; };
constexpr Op ring(int s, int k) {
  if (s == -2) { const Op o[4] = {{1, 0}, {2, 0}, {3, 0}, {1, 1}}; return o[k]; }
  if (s == -1) { const Op o[4] = {{1, 2}, {2, 1}, {3, 1}, {0, 0}}; return o[k]; }
  if (s == 0) { const Op o[4] = {{1, 3}, {1, 4}, {2, 2}, {0, 0}}; return o[k]; }
  if (s == 1) { const Op o[4] = {{3, 2}, {1, 5}, {1, 6}, {0, 0}}; return o[k]; }
  if (s == 2) { const Op o[4] = {{2, 3}, {3, 3}, {1, 7}, {0, 0}}; return o[k]; }
  if (s > 14 || k > 1) return Op{0, 0};
  if (s & 1) return k == 0 ? Op{1, s + 5} : Op{2, s / 2 + 3};
  return k == 0 ? Op{3, s / 2 + 2} : Op{1, s + 5};
}
constexpr int extras(int role, int s) {
  if (s == -2) return 1;
  if (role == 0) return s == 15 ? 6 : (s == 17 || s == 18) ? 5 : 0;
  return (s == 16 || s == 17 || s == 19) ? 5 : 0;
}
constexpr int ring_wait(int role, int s) {
  int n = 0;
  for (int q = -2; q < s; ++q) {
    for (int k = 0; k < 4; ++k) {
      Op o = ring(q, k);
      n += o.kind == 0 ? 0 : o.kind == 3 ? 1 : 2;
      if ((o.kind == 1 && o.idx == s + 1) || (o.kind == 3 && o.idx == (s + 1) / 2)) n = 0;
    }
    n += extras(role, q);
  }
  return n;
}
template <int N> struct IC { static constexpr int v = N; };
template <int A, int B, class F> DI void sfor(F&& f) { if constexpr (A < B) { f(IC<A>{}); sfor<A + 1, B>(f); } }

struct P {
  const void *x, *wqkv, *wo, *wg, *wu, *wd;
  void* out;
  char* ws;
};

template <int TYPE, int ROLE>
DI void body(const P& p, int job, int wave) {
  constexpr bool ATT = TYPE == 1;
  constexpr int NT = ATT ? 3 : 6, NC = ATT ? 8 : 4;
  TSDECL; TSR(0); TS(1);
  int lane, g, r16, q4, p4;
  auto relane = [&]() { lane = lane_id(); g = lane >> 4; r16 = lane & 15; q4 = r16 >> 2; p4 = lane & 3; };
  relane();
  const int b = blockIdx.x, xcd = b & 7, bi = b >> 3;
  const __amdgpu_buffer_rsrc_t rw = __builtin_amdgcn_make_buffer_rsrc(p.ws, 0, WS_BYTES, 0x00020000);
  const i32x4 sa_ = mk_srd(p.ws, WS_BYTES);
  const i32x4 sx = mk_srd((const char*)p.x - 2048, S_ * D_ * 2 + 2048);

  const int sidx = (xcd >> 2) * 1024 + (xcd & 3) * 256 + bi * 8 + wave;
  const int stok = 2 * (sidx & 1023) + (sidx >> 10);
  unsigned ev, sc_;
  u32x4 sa;
  asm volatile("buffer_load_dword %0, off, %5, %6 sc1\n\tbuffer_load_dwordx4 %1, %3, %7, %8 offen offset:2048\n\t"
               "buffer_load_dword %2, %4, %7, %8 offen offset:3072"
               : "=&v"(ev), "=&v"(sa), "=&v"(sc_) : "v"(lane * 16), "v"(lane * 4), "s"(sa_), "s"(CTL), "s"(sx), "s"(stok * 1280) : "memory");
  SB();

  int tile0 = 0, tlo = 0, thi = 127, slice = 0, G = 0;
  if (ATT) { G = job & 7; int pp = job >> 3, par = pp / 6; pp -= par * 6; tile0 = par * 64 + 11 * pp - 1; tlo = par * 64; thi = par * 64 + 63; }
  else { int pn = job / 14; slice = job - pn * 14; tile0 = 12 * pn; }
  const int tg = ATT ? (wave >> 1) : ROLE, cg = ATT ? (wave & 1) : (wave & 3);

  unsigned xo[3], wo_[2];
  i32x4 sw;
  constexpr int wstep = ATT ? 32 * 3840 : 32 * 3456;
  if (TYPE != 2) {
    if (ATT) {
      sw = mk_srd(p.wqkv, D_ * 1920 * 2);
#pragma unroll
      for (int i = 0; i < 2; ++i) {
        int idx = min((wave * 2 + i) * 64 + lane, 959);
        int m = idx / 320, rem = idx - m * 320, rho = rem / 10, c = rem - rho * 10;
        int k = (rho & 19) | ((rho & 4) << 1) | ((rho & 8) >> 1);
        wo_[i] = k * 3840 + m * 1280 + G * 160 + c * 16 - i * 1024;
      }
    } else {
      sw = mk_srd(ROLE ? p.wu : p.wg, D_ * DF_ * 2);
      int lc = (wave >> 1) & 1; if (slice == 13) lc = 0;
#pragma unroll
      for (int i = 0; i < 2; ++i) {
        int row = ((wave & 1) * 2 + i) * 8 + (lane >> 3);
        int f = ((row >> 1) & 1) | (((row >> 3) & 1) << 1);
        wo_[i] = row * 3456 + slice * 256 + lc * 128 + (((lane & 7) ^ (f << 1)) << 4) - i * 1024;
      }
    }
#pragma unroll
    for (int i = 0; i < 3; ++i) {
      int row = (wave * 3 + i) * 8 + (lane >> 3);
      int tt = min(max(tile0 + (row >> 4), tlo), thi);
      int tok = 2 * ((tt & 63) * 16 + (row & 15)) + (tt >> 6);
      xo[i] = tok * (D_ * 2) + (((lane & 7) ^ ((row >> 1) & 7)) << 4) + 2048 - i * 1024;
    }
  }
  const unsigned wdst = wave * 2048, xdst = wave * 3072;
  const unsigned long long xmask = wave == 0 ? ~0xffull : ~0ull;
  auto ring_ops = [&](auto sc) {
    constexpr int s = decltype(sc)::v;
    sfor<0, 4>([&](auto kc) {
      constexpr Op o = ring(s, decltype(kc)::v);
      if constexpr (o.kind == 1) dma2<wslot(o.idx), o.idx * wstep>(sw, wo_[0], wo_[1], wdst);
      if constexpr (o.kind == 2) {
        if constexpr (ATT && ROLE == 0) dma2m<xslot(o.idx), o.idx * 128>(sx, xo[0], xo[1], xdst, xmask);
        else dma2<xslot(o.idx), o.idx * 128>(sx, xo[0], xo[1], xdst);
      }
      if constexpr (o.kind == 3) dma1c<xslot(o.idx), o.idx * 128>(sx, xo[2], xdst);
    });
  };
  if (TYPE != 2) ring_ops(IC<-2>{});
  SB();
  if (ATT && ROLE == 0 && wave == 0 && lane < 24) {
    u32x4 o = {ONES, ONES, ONES, ONES};
    *(LDS_AS u32x4*)(L_X + (lane >> 3) * XSLOT + (lane & 7) * 16) = o;
  }

  const int rt = xcd * 4 + (bi >> 3), ct = bi & 7, wr = wave & 3;
  const int rot = ct < 6 ? ct : ct - 6;
  const int kh0 = ROLE ? 28 + 6 * wr : 7 * wr;
  auto kf_of = [&](int i) {
    if (i < 6) { int j = i + rot; return kh0 + (j >= 6 ? j - 6 : j); }
    if (ROLE == 0) return i == 6 ? kh0 + 6 : i < 9 ? 54 + 2 * wr + (i - 7) : 70 + wr;
    return i < 8 ? 62 + 2 * wr + (i - 6) : 52 + (wr & 1);
  };
  constexpr int SB0[4] = {ROLE ? 0 : 81920, ROLE ? 20480 : 102400, ROLE ? 61440 : 40960, ROLE ? 143360 : 122880};
  const unsigned sl_ = wr * 5120;
  const i32x4 sd = mk_srd(p.wd, DF_ * D_ * 2), so = mk_srd(p.wo, D_ * D_ * 2);
  unsigned v2[5];
  auto mk_v2 = [&](int A, int B) {
    const int l = lane_id();
#pragma unroll
    for (int i = 0; i < 5; ++i) {
      int idx = i * 64 + l, rho = idx / 10, c = idx - rho * 10;
      v2[i] = ((rho >> 4) * A + ((rho >> 2) & 3) * B + (rho & 3)) * 1280 + c * 16 - min(i, 3) * 1024;
    }
  };
  auto dmaH = [&](int i, int slot) { dma5(sd, v2, kf_of(i) * (32 * 1280) + ct * 160, SB0[slot] + sl_); };
  bf16x8 F[10][5];
  auto rdF = [&](int i, int slot) {
    const int l = lane_id();
    const unsigned a = opq(SB0[slot] + sl_ + (l >> 2) * 160 + (l & 3) * 8);
#pragma unroll
    for (int n = 0; n < 5; ++n) F[i][n] = lds_tr(a + n * 32, 16 * 160);
  };

  asm volatile("s_waitcnt vmcnt(%3)" : "+v"(ev), "+v"(sa), "+v"(sc_) : "i"(TYPE == 2 ? 0 : 7) : "memory");
  const unsigned ep = __builtin_amdgcn_readfirstlane(ev), tag = ep * 0x9E3779B1u + 0x7F4A7C15u;
  {
    float e[10] = {blo(sa[0]), bhi(sa[0]), blo(sa[1]), bhi(sa[1]), blo(sa[2]), bhi(sa[2]), blo(sa[3]), bhi(sa[3]), blo(sc_), bhi(sc_)};
    float s = 0;
#pragma unroll
    for (int i = 0; i < 10; ++i) s += e[i];
    float nm = wave_sum(s) * (-1.0f / D_), q = 0;
#pragma unroll
    for (int i = 0; i < 10; ++i) { float d = e[i] + nm; q = fmaf(d, d, q); }
    float rs = __builtin_amdgcn_rsqf(wave_sum(q) * (1.0f / D_) + 1e-5f);
    st4l(u32x4{__float_as_uint(rs), tag, __float_as_uint(nm), tag}, sa_, GRN + sidx * 16);
  }
  SB();
  TS(2);

  if constexpr (TYPE == 2) {
    mk_v2(16, 4);
    dmaH(0, 0); dmaH(1, 1); dmaH(2, 2);
    if (b == 249) {
#pragma nounroll
      for (int it = 0; it < SPIN_CAP; ++it) {
        u32x4 q[4];
        ld16c(q, sa_, lane * 16, GRN + wave * 4096);
        got4<0>(q);
        if (__all(q[0][1] == tag && q[1][1] == tag && q[2][1] == tag && q[3][1] == tag)) break;
        __builtin_amdgcn_s_sleep(8);
      }
      BAR();
      if (wave == 0 && lane == 0) __builtin_amdgcn_raw_buffer_store_b32(ep + 1, rw, 0, CTL, 16);
    }
    WAIT_V(0);
    rdF(0, 0); rdF(1, 1); rdF(2, 2);
    WAIT_L0();
    dmaH(3, 0); dmaH(4, 1); dmaH(5, 2);
    WAIT_V(0);
    rdF(3, 0); rdF(4, 1); rdF(5, 2);
    WAIT_L0();
  } else {
    unsigned xb[2], wb[3];
    xb[0] = opq(L_X + (tg * NT * 16 + r16) * 128 + ((g ^ (r16 >> 1)) << 4));
    xb[1] = opq(xb[0] ^ 64);
    if (ATT) {
      unsigned rb = ((g >> 1) * 16 + (g & 1) * 4 + q4) * 160;
      wb[0] = rb + cg * 80 + p4 * 8;
      wb[1] = rb + cg * 80 + (p4 >> 1) * 5120 + (p4 & 1) * 40 + 32;
      wb[2] = rb + 10240 + p4 * 40 + 32;
    } else {
      int row = 8 * g + q4, f = ((row >> 1) & 1) | (((row >> 3) & 1) << 1);
      int uA = (2 * (cg & 1)) | tg;
      unsigned rb = (cg >> 1) * 4096 + row * 128 + p4 * 8;
      wb[0] = rb + ((uA ^ f) << 5);
      wb[1] = rb + ((uA ^ 1 ^ f) << 5);
      wb[2] = 0;
    }
    f32x4 acc[NT][NC], cso[2];
    bf16x8 xf[2][NT], wf[2][NC];
    const f32x4 z4 = {0, 0, 0, 0};
    const bf16x8 ones8 = mk8(ONES, ONES, ONES, ONES);
    auto reads = [&](int bf, int s) {
      const bool hi = s % 5 == 4;
      const unsigned wa = hi ? up64k(wb[0]) : wb[0] + wslot(s), wa1 = hi ? up64k(wb[1]) : wb[1] + wslot(s);
      const unsigned wa2 = !ATT ? 0 : hi ? up64k(wb[2]) : wb[2] + wslot(s), xa = xb[s & 1] + (xslot(s >> 1) - L_X);
      if (ATT) {
        wf[bf][0] = lds_tr(wa, 1280);
        xf[bf][0] = lds_b128(xa);
        wf[bf][1] = lds_tr(wa + 40, 1280);
        wf[bf][2] = lds_tr(wa + 5120, 1280);
        wf[bf][3] = lds_tr(wa + 5160, 1280);
        wf[bf][4] = lds_tr(wa1, 1280);
        wf[bf][5] = lds_tr(wa + 10240, 1280);
        wf[bf][6] = lds_tr(wa + 10280, 1280);
        wf[bf][7] = lds_tr(wa2, 1280);
      } else {
        wf[bf][0] = lds_tr(wa, 512);
        xf[bf][0] = lds_b128(xa);
        wf[bf][1] = lds_tr(wa + 8192, 512);
        wf[bf][2] = lds_tr(wa1, 512);
        wf[bf][3] = lds_tr(wa1 + 8192, 512);
      }
#pragma unroll
      for (int t = 1; t < NT; ++t) xf[bf][t] = lds_b128(xa + t * 2048);
    };
    auto mm = [&](int bf, bool first, int i0, int i1) {
#pragma unroll
      for (int i = i0; i < i1; ++i) {
        const int t = i / NC, c = i % NC;
        f32x4 ci = first ? z4 : acc[t][c];
        acc[t][c] = (ATT && c >= 5) ? mfma(xf[bf][t], wf[bf][c], ci) : mfma(wf[bf][c], xf[bf][t], ci);
        SB();
      }
    };
    auto mcs = [&](int bf, bool first) {
      if (!ATT) { cso[0] = mfma(wf[bf][0], ones8, first ? z4 : cso[0]); cso[1] = mfma(wf[bf][1], ones8, first ? z4 : cso[1]); SB(); }
    };
    auto pin = [&]() {
#pragma unroll
      for (int t = 0; t < NT; ++t)
#pragma unroll
        for (int c = 0; c < NC; ++c) PIN(acc[t][c]);
      if (!ATT) { PIN(cso[0]); PIN(cso[1]); }
    };
    u32x4 gr;
    unsigned goff;
    auto late = [&](int s) {
      if (s == 14) mk_v2(16, 4);
      if (ROLE == 0) {
        if (s == 15) {
          const unsigned grow = min(wave * 64 + lane_id(), 191);
          goff = (min(max(tile0 + (int)(grow >> 4), tlo), thi) * 16 + (grow & 15)) * 16;
          gr = ld4c(sa_, goff, GRN); dmaH(0, 0);
        }
        if (s == 17) {
          got<5>(gr);
#pragma nounroll
          for (int it = 0; it < SPIN_CAP && !__all(gr[1] == tag && gr[3] == tag); ++it) {
            __builtin_amdgcn_s_sleep(2);
            gr = ld4c(sa_, goff, GRN);
            got<0>(gr);
          }
          *(LDS_AS u32x2*)(L_ST + (wave * 64 + lane_id()) * 8) = u32x2{gr[0], gr[2]};
          SB();
          dmaH(1, 1);
        }
        if (s == 18) dmaH(2, 2);
      } else {
        if (s == 16) dmaH(0, 0);
        if (s == 17) dmaH(1, 1);
      }
    };

    WAIT_VL(ring_wait(ROLE, -1)); BAR();
    TS(3);
    reads(0, 0);
    SB();
    ring_ops(IC<-1>{});
    if (ROLE == 1) __builtin_amdgcn_s_setprio(PRIOB);
    sfor<0, 19>([&](auto sc) {
      constexpr int s = decltype(sc)::v, cur = ROLE == 0 ? (s & 1) : 0, nxt = ROLE == 0 ? (cur ^ 1) : 0;
      WAIT_VL(ring_wait(ROLE, s)); BAR();
      SB();
      if (ROLE == 0) {
        reads(nxt, s + 1); SB();
        ring_ops(sc); late(s); SB();
        mcs(cur, s == 0); mm(cur, s == 0, 0, 24); pin();
      } else {
        mcs(cur, s == 0); mm(cur, s == 0, 0, 24); pin();
        reads(nxt, s + 1); SB();
        ring_ops(sc); late(s); SB();
      }
    });
    constexpr int lb = ROLE == 0 ? 1 : 0;
    relane();
    if (ATT) {
      if (ROLE == 0) {
        mm(lb, false, 0, 8);
        if (tg == 0) {
          if (r16 == 0) {
#pragma unroll
            for (int c = 0; c < 5; ++c) *(LDS_AS f32x4*)(L_CS + ((cg * 8 + c) * 16 + 4 * g) * 4) = acc[0][c];
          }
          if (lane < 16) {
#pragma unroll
            for (int c = 5; c < 8; ++c) *(LDS_AS float*)(L_CS + ((cg * 8 + c) * 16 + lane) * 4) = acc[0][c][0];
          }
        }
      }
    } else {
      mcs(lb, false);
      if (r16 == 0) { *(LDS_AS f32x4*)(L_CS + wave * 128 + g * 16) = cso[0]; *(LDS_AS f32x4*)(L_CS + wave * 128 + 64 + g * 16) = cso[1]; }
    }
    WAIT_L0(); BAR();
    SB();
    mm(lb, false, (ATT && ROLE == 0) ? 8 : 0, 24); pin();
    if (ROLE == 1) { dmaH(2, 2); __builtin_amdgcn_s_setprio(0); }
    TS(4);
    WAIT_V(ROLE == 0 ? 0 : 5);
    rdF(0, 0); rdF(1, 1);
    if (ROLE == 0) rdF(2, 2);
    WAIT_L0();
    dmaH(3, 0); dmaH(4, 1);
    if (ROLE == 0) dmaH(5, 2);
    SB();
    relane();

    if (!ATT) {
      f32x4 cs[4] = {cso[0], cso[1], lds_f4(L_CS + (wave ^ 4) * 128 + g * 16), lds_f4(L_CS + (wave ^ 4) * 128 + 64 + g * 16)};
      const int kf = slice * 4 + cg;
#pragma unroll
      for (int t = 0; t < NT; ++t) {
        f32x2 ab = *(LDS_AS f32x2*)(L_ST + ((tg * NT + t) * 16 + r16) * 8);
        const float a2 = ab[0] * ab[0], k1 = ab[0] * -2.3022082f, k3 = a2 * ab[0] * -0.10294324f, ia2 = __builtin_amdgcn_rcpf(a2);
        float h[2][4];
#pragma unroll
        for (int s2 = 0; s2 < 2; ++s2)
#pragma unroll
          for (int r = 0; r < 4; ++r) {
            float gv = fmaf(ab[1], cs[2 * s2][r], acc[t][2 * s2][r]);
            float uv = fmaf(ab[1], cs[2 * s2 + 1][r], acc[t][2 * s2 + 1][r]);
            float e = __builtin_amdgcn_exp2f(gv * fmaf(gv * gv, k3, k1));
            h[s2][r] = gv * uv * __builtin_amdgcn_rcpf(fmaf(e, ia2, ia2));
          }
        unsigned a0 = pk(h[0][0], h[0][1]), a1 = pk(h[0][2], h[0][3]), b0 = pk(h[1][0], h[1][1]), b1 = pk(h[1][2], h[1][3]);
        u32x4 v = ROLE ? u32x4{b0, b1, a0, a1} : u32x4{a0, a1, b0, b1};
        int tt = tile0 + tg * NT + t;
        unsigned so_ = (tt <= 127 && kf < 54) ? (unsigned)(((tt >> 2) * NKF + kf) * 4 + (tt & 3)) * 1024u : DUM + (b * 8 + wave) * 1024u;
        st4c(v, sa_, lane * 16, so_);
        if (ROLE == 1 && t == 2) { SB(); WAIT_V(13); rdF(2, 2); WAIT_L0(); dmaH(5, 2); SB(); }
      }
    } else {
      constexpr float QS = 0.22360679775f * 1.44269504089f;
      f32x4 cs[5]; float cv[3];
#pragma unroll
      for (int c = 0; c < 5; ++c) cs[c] = lds_f4(L_CS + ((cg * 8 + c) * 16 + 4 * g) * 4);
#pragma unroll
      for (int c = 0; c < 3; ++c) cv[c] = *(LDS_AS float*)(L_CS + ((cg * 8 + 5 + c) * 16 + r16) * 4);
      u32x4 Q[3][2], K[4][2];
      u32x8 V[3];
      const unsigned mh0 = g == 0 ? ~0u : 0u, mh1 = g == 1 ? ~0u : 0u;
      auto ln = [&](int t) {
        f32x2 ab = *(LDS_AS f32x2*)(L_ST + ((tg * 3 + t) * 16 + r16) * 8);
        f32x4 v0 = lds_f4(L_ST + ((tg * 3 + t) * 16 + 4 * g) * 8), v1 = lds_f4(L_ST + ((tg * 3 + t) * 16 + 4 * g) * 8 + 16);
        const float aq = ab[0] * QS, at = g < 2 ? aq : ab[0];
        unsigned w[5][2];
#pragma unroll
        for (int c = 0; c < 5; ++c) {
          const float a = c < 2 ? aq : (c < 4 ? ab[0] : at);
          float y[4];
#pragma unroll
          for (int r = 0; r < 4; ++r) y[r] = a * fmaf(ab[1], cs[c][r], acc[t][c][r]);
          w[c][0] = pk(y[0], y[1]); w[c][1] = pk(y[2], y[3]);
        }
        u32x2 s0 = __builtin_amdgcn_permlane32_swap(w[4][0], w[4][0], false, false);
        u32x2 s1 = __builtin_amdgcn_permlane32_swap(w[4][1], w[4][1], false, false);
        Q[t][0] = u32x4{w[0][0], w[0][1], s0[0] & mh0, s1[0] & mh0};
        Q[t][1] = u32x4{w[1][0], w[1][1], s0[0] & mh1, s1[0] & mh1};
        K[t + 1][0] = u32x4{w[2][0], w[2][1], s0[1], s1[1]};
        K[t + 1][1] = u32x4{w[3][0], w[3][1], s0[1], s1[1]};
#pragma unroll
        for (int c = 0; c < 3; ++c) {
          V[c][2 * t + 2] = pk(v0[0] * fmaf(v0[1], cv[c], acc[t][5 + c][0]), v0[2] * fmaf(v0[3], cv[c], acc[t][5 + c][1]));
          V[c][2 * t + 3] = pk(v1[0] * fmaf(v1[1], cv[c], acc[t][5 + c][2]), v1[2] * fmaf(v1[3], cv[c], acc[t][5 + c][3]));
        }
      };
      ln(2);
      {
        unsigned ex = L_EX + wave * 3072 + lane * 16;
        *(LDS_AS u32x4*)(ex) = K[3][0];
        *(LDS_AS u32x4*)(ex + 1024) = u32x4{K[3][1][0], K[3][1][1], V[0][6], V[0][7]};
        *(LDS_AS u32x4*)(ex + 2048) = u32x4{V[1][6], V[1][7], V[2][6], V[2][7]};
      }
      ln(1); ln(0);
      if (ROLE == 1) { SB(); WAIT_V(10); rdF(2, 2); WAIT_L0(); dmaH(5, 2); SB(); }
      f32x4 Cc, Cp, Cp1;
      const bool hp1 = tile0 + tg * 3 + 1 > tlo;
#pragma unroll
      for (int r = 0; r < 4; ++r) {
        int kk = 4 * g + r;
        Cc[r] = (kk <= r16 && kk + 7 >= r16) ? 0.f : -INFINITY;
        Cp[r] = kk >= r16 + 9 ? 0.f : -INFINITY;
        Cp1[r] = hp1 ? Cp[r] : -INFINITY;
      }
      const unsigned mp0 = (4 * g > r16 ? 0xffffu : 0u) | (4 * g + 1 > r16 ? 0xffff0000u : 0u);
      const unsigned mp1 = (4 * g + 2 > r16 ? 0xffffu : 0u) | (4 * g + 3 > r16 ? 0xffff0000u : 0u);
      auto logits = [&](int t, int h, f32x4& s, float& m) {
        f32x4 sp = mfma(as8(K[t][h]), as8(Q[t][h]), t == 1 ? Cp1 : Cp);
        f32x4 sc = mfma(as8(K[t + 1][h]), as8(Q[t][h]), Cc);
#pragma unroll
        for (int r = 0; r < 4; ++r) s[r] = fmaxf(sp[r], sc[r]);
        m = fmaxf(fmaxf(s[0], s[1]), fmaxf(s[2], s[3]));
      };
      auto expo = [&](f32x4& s, float m, float& sum) {
#pragma unroll
        for (int r = 0; r < 4; ++r) s[r] = __builtin_amdgcn_exp2f(s[r] - m);
        sum = (s[0] + s[1]) + (s[2] + s[3]);
      };
      auto vsub = [&](const u32x8& v, int t) {
        return t == 0 ? __builtin_shufflevector(v, v, 0, 1, 2, 3) : t == 1 ? __builtin_shufflevector(v, v, 2, 3, 4, 5) : __builtin_shufflevector(v, v, 4, 5, 6, 7);
      };
      auto outp = [&](int t, f32x4 (&s)[2], float (&inv)[2]) {
        f32x4 o1[2], o2[2];
#pragma unroll
        for (int h = 0; h < 2; ++h) {
          unsigned p0 = pk(s[h][0] * inv[h], s[h][1] * inv[h]), p1 = pk(s[h][2] * inv[h], s[h][3] * inv[h]);
          bf16x8 pf = mk8(p0 & mp0, p1 & mp1, p0 & ~mp0, p1 & ~mp1);
          o1[h] = mfma(as8(vsub(V[h], t)), pf, z4);
          o2[h] = mfma(as8(vsub(V[2], t)), pf, z4);
        }
        const int tt = tile0 + tg * 3 + t;
        const bool ok = !(tg == 0 && t == 0) && tt <= thi;
        const unsigned base = (unsigned)((tt >> 2) * NKF * 4 + (tt & 3)) * 1024u, dum = DUM + (b * 8 + wave) * 1024u;
        st4c(u32x4{pk(o1[0][0], o1[0][1]), pk(o1[0][2], o1[0][3]), pk(o1[1][0], o1[1][1]), pk(o1[1][2], o1[1][3])},
             sa_, lane * 16, ok ? base + (54 + 2 * G + cg) * 4096u : dum);
        const bool od = g & 1;
        u32x2 v = {pk(od ? o2[1][0] : o2[0][0], od ? o2[1][1] : o2[0][1]), pk(od ? o2[1][2] : o2[0][2], od ? o2[1][3] : o2[0][3])};
        if ((g >> 1) == cg) __builtin_amdgcn_raw_buffer_store_b64(v, rw, lane * 16, ok ? base + (70 + (G >> 1)) * 4096u + (G & 1) * 8 : dum, 16);
      };
      {
        f32x4 s2[2], s1[2]; float m[4], sm[4], i2[2], i1[2];
        logits(2, 0, s2[0], m[0]); logits(2, 1, s2[1], m[1]); logits(1, 0, s1[0], m[2]); logits(1, 1, s1[1], m[3]);
        f32x2 x = sw16(m[0], m[1]), y = sw16(m[2], m[3]);
        x = sw32(fmaxf(x[0], x[1]), fmaxf(y[0], y[1]));
        float z = fmaxf(x[0], x[1]);
        x = sw32(z, z); y = sw16(x[1], x[1]); x = sw16(x[0], x[0]);
        expo(s2[0], x[0], sm[0]); expo(s2[1], x[1], sm[1]); expo(s1[0], y[0], sm[2]); expo(s1[1], y[1], sm[3]);
        x = sw16(sm[0], sm[1]); y = sw16(sm[2], sm[3]);
        x = sw32(x[0] + x[1], y[0] + y[1]);
        z = __builtin_amdgcn_rcpf(x[0] + x[1]);
        x = sw32(z, z); y = sw16(x[1], x[1]); x = sw16(x[0], x[0]);
        i2[0] = x[0]; i2[1] = x[1]; i1[0] = y[0]; i1[1] = y[1];
        WAIT_L0(); BAR();
        unsigned ey = L_EX + ((wave + 6) & 7) * 3072 + lane * 16;
        u32x4 a = *(LDS_AS u32x4*)(ey), bb = *(LDS_AS u32x4*)(ey + 1024), c = *(LDS_AS u32x4*)(ey + 2048);
        outp(2, s2, i2); outp(1, s1, i1);
        K[0][0] = a; K[0][1] = u32x4{bb[0], bb[1], a[2], a[3]};
        V[0][0] = bb[2]; V[0][1] = bb[3]; V[1][0] = c[0]; V[1][1] = c[1]; V[2][0] = c[2]; V[2][1] = c[3];
      }
      {
        f32x4 s0[2]; float m[2], sm[2], i0[2];
        logits(0, 0, s0[0], m[0]); logits(0, 1, s0[1], m[1]);
        f32x2 x = sw16(m[0], m[1]);
        float z = fmaxf(x[0], x[1]);
        x = sw32(z, z); z = fmaxf(x[0], x[1]); x = sw16(z, z);
        expo(s0[0], x[0], sm[0]); expo(s0[1], x[1], sm[1]);
        x = sw16(sm[0], sm[1]); z = x[0] + x[1];
        x = sw32(z, z); z = __builtin_amdgcn_rcpf(x[0] + x[1]); x = sw16(z, z);
        i0[0] = x[0]; i0[1] = x[1];
        outp(0, s0, i0);
      }
    }
    SB();
    TS(5);
    WAIT_V((ROLE == 1 && !ATT) ? 3 : 6);
    rdF(3, 0); rdF(4, 1); rdF(5, 2);
    asm volatile("s_waitcnt vmcnt(0) lgkmcnt(0)" ::: "memory");
    WAIT_VL(0);
    BAR();
    if (ROLE == 0 && wave == 0 && lane == 0) __builtin_amdgcn_raw_buffer_store_b32(tag, rw, 0, FLG + (ATT ? 154 + job : job) * 64, 16);
  }
  TS(6);

  relane();
  unsigned fo;
  {
    const int pn = rt / 3, par = rt >> 4, lt = (rt & 15) * 4, plo = par * 6 + lt / 11, phi = par * 6 + (lt + 3) / 11;
    int l = lane & 3, fi;
    int sl = ROLE == 0 ? min((kh0 >> 2) + l, (kh0 + 6) >> 2) : (l == 3 ? 13 : min((kh0 >> 2) + l, (kh0 + 5) >> 2));
    int gg = ROLE == 0 ? (l == 0 ? wr : 2 * wr + (l & 1)) : 4 + wr;
    fi = lane < 4 ? pn * 14 + sl : 154 + ((lane & 4) ? plo : phi) * 8 + gg;
    fo = fi * 64;
  }
  const unsigned l16 = lane * 16;
  unsigned fv = ld1c(sa_, fo, FLG);
  if (ROLE == 0) { dmaH(6, 0); mk_v2(20, 4); dma5(so, v2, (kf_of(7) - 54) * (40 * 1280) + ct * 160, SB0[1] + sl_); }
  else { dmaH(8, 2); mk_v2(20, 4); dma5(so, v2, (kf_of(6) - 54) * (40 * 1280) + ct * 160, SB0[0] + sl_); }
  got<10>(fv);
  unsigned long long bad = __ballot(fv != tag);
  auto poll = [&](unsigned long long m) {
#pragma nounroll
    for (int it = 0; it < SPIN_CAP && (bad & m); ++it) {
      fv = ld1c(sa_, fo, FLG);
      got<0>(fv);
      bad = __ballot(fv != tag);
    }
  };
  poll(15);
  TS(7);
  u32x4 A[10][4];
  f32x4 o[4][5];
  auto ldA = [&](int i) { ld16c(A[i], sa_, l16, (unsigned)(rt * NKF + kf_of(i)) * 4096u); };
  auto mm2 = [&](int i) {
#pragma unroll
    for (int t = 0; t < 4; ++t)
#pragma unroll
      for (int n = 0; n < 5; ++n) o[t][n] = mfma(F[i][n], as8(A[i][t]), i == 0 ? f32x4{0, 0, 0, 0} : o[t][n]);
#pragma unroll
    for (int t = 0; t < 4; ++t)
#pragma unroll
      for (int n = 0; n < 5; ++n) PIN(o[t][n]);
    SB();
  };
  ldA(0); ldA(1);
  if (ROLE == 0) dma5(so, v2, (kf_of(8) - 54) * (40 * 1280) + ct * 160, SB0[2] + sl_);
  else dma5(so, v2, (kf_of(7) - 54) * (40 * 1280) + ct * 160, SB0[1] + sl_);
  if (ROLE == 0) { mk_v2(80, 20); dma5(so, v2, (wr * 160 + 16) * 1280 + ct * 160, SB0[3] + sl_); }
  constexpr int ND = ROLE == 0 ? 10 : 5;
  constexpr int i6 = ROLE == 0 ? 6 : 8, i7 = ROLE == 0 ? 7 : 6, i8 = ROLE == 0 ? 8 : 7;
  constexpr int s6 = ROLE == 0 ? 0 : 2, s7 = ROLE == 0 ? 1 : 0, s8 = ROLE == 0 ? 2 : 1;
  got4<4 + ND>(A[0]); mm2(0); ldA(2);
  got4<ND + 4>(A[1]); mm2(1); ldA(3); ldA(4);
  got4<8>(A[2]); mm2(2); ldA(5);
  got4<8>(A[3]); mm2(3); ldA(i6); rdF(i6, s6);
  poll(0xff0);
  got4<8>(A[4]); mm2(4); ldA(i7); rdF(i7, s7);
  got4<8>(A[5]); mm2(5); ldA(i8); rdF(i8, s8);
  if (ROLE == 0) { ldA(9); rdF(9, 3); }
  relane();
  const int ot = wave >> 1, opr = wave & 1;
  const int ott = rt * 4 + ot, otok = 2 * ((ott & 63) * 16 + r16) + (ott >> 6);
  const unsigned eo = (otok * D_ + ct * 80 + opr * 32 + g * 8) * 2, eo2 = (otok * D_ + ct * 80 + 64 + opr * 8 + (g & 1) * 4) * 2;
  u32x4 xr; u32x2 xr2;
  asm volatile("buffer_load_dwordx4 %0, %2, %4, 0 offen offset:2048\n\tbuffer_load_dwordx2 %1, %3, %4, 0 offen offset:2048"
               : "=&v"(xr), "=&v"(xr2) : "v"(eo), "v"(eo2), "s"(sx) : "memory");
  if (ROLE == 0) {
    got4<14>(A[6]); mm2(6); got4<10>(A[7]); mm2(7); got4<6>(A[8]); mm2(8); got4<2>(A[9]); mm2(9);
  } else {
    got4<10>(A[8]); if (wr < 2) mm2(8);
    got4<6>(A[6]); mm2(6); got4<2>(A[7]); mm2(7);
  }
  TS(8);
#pragma unroll
  for (int t = 0; t < 4; ++t)
#pragma unroll
    for (int n = 0; n < 5; ++n) *(LDS_AS f32x4*)(opq(SB0[t] + sl_ + l16) + n * 1024) = o[t][n];
  WAIT_L0(); BAR();
  TS(9);
  {
    const unsigned ba = ot == 0 ? 81920 : ot == 1 ? 102400 : ot == 2 ? 40960 : 122880;
    const unsigned bb = ot == 0 ? 0 : ot == 1 ? 20480 : ot == 2 ? 61440 : 143360;
    const unsigned l1 = (opr * 2 + (g >> 1)) * 1024 + (g & 1) * 512 + r16 * 16, l2 = 4096 + (opr * 2 + (g & 1)) * 256 + r16 * 16;
    f32x4 s0 = {0, 0, 0, 0}, s1 = s0, s2 = s0;
#pragma unroll
    for (int v = 0; v < 8; ++v) {
      const unsigned sb = (v < 4 ? ba : bb) + (v & 3) * 5120;
      s0 += lds_f4(sb + l1); s1 += lds_f4(sb + l1 + 256); s2 += lds_f4(sb + l2);
    }
    got<0>(xr, xr2);
    u32x4 r;
    r[0] = pk(s0[0] + blo(xr[0]), s0[1] + bhi(xr[0])); r[1] = pk(s0[2] + blo(xr[1]), s0[3] + bhi(xr[1]));
    r[2] = pk(s1[0] + blo(xr[2]), s1[1] + bhi(xr[2])); r[3] = pk(s1[2] + blo(xr[3]), s1[3] + bhi(xr[3]));
    *(u32x4*)((char*)p.out + eo) = r;
    u32x2 r2 = {pk(s2[0] + blo(xr2[0]), s2[1] + bhi(xr2[0])), pk(s2[2] + blo(xr2[1]), s2[3] + bhi(xr2[1]))};
    if (lane < 32) *(u32x2*)((char*)p.out + eo2) = r2;
  }
  TS(10); TSR(11); TSOUT();
}

__global__ __launch_bounds__(512, 2) void k_fused(P p) {
  const int wave = __builtin_amdgcn_readfirstlane(threadIdx.x >> 6);
  const int b = blockIdx.x, xcd = b & 7, i = b >> 3, th = xcd >> 2, wq = xcd & 3;
  if (i < 12) {
    int job = (th * 6 + (i >> 1)) * 8 + wq * 2 + (i & 1);
    if (wave < 4) body<1, 0>(p, job, wave); else body<1, 1>(p, job, wave);
  } else if (i - 12 < (wq == 0 ? 20 : 19)) {
    int n = wq * 19 + min(wq, 1) + (i - 12), sl, pn;
    if (th == 0) { if (n < 42) { sl = n / 6; pn = n - sl * 6; } else { int m = n - 42; sl = m / 5; pn = m - sl * 5; sl += 7; } }
    else { if (n < 35) { sl = n / 5; pn = 6 + n - sl * 5; } else { int m = n - 35; sl = m / 6; pn = 5 + m - sl * 6; sl += 7; } }
    if (wave < 4) body<0, 0>(p, pn * 14 + sl, wave); else body<0, 1>(p, pn * 14 + sl, wave);
  } else {
    if (wave < 4) body<2, 0>(p, 0, wave); else body<2, 1>(p, 0, wave);
  }
}

extern "C" void kernel_launch(void* const* d_in, const int* in_sizes, int n_in, void* d_out, int out_size, void* d_ws,
                              size_t ws_size, hipStream_t stream) {
  P p;
  p.x = d_in[0]; p.wqkv = d_in[1]; p.wo = d_in[2]; p.wg = d_in[4]; p.wu = d_in[5]; p.wd = d_in[6];
  p.out = d_out; p.ws = (char*)d_ws;
  (void)hipFuncSetAttribute((const void*)k_fused, hipFuncAttributeMaxDynamicSharedMemorySize, LDS_BYTES);
  k_fused<<<256, 512, LDS_BYTES, stream>>>(p);
}
```

```cpp
#include <hip/hip_runtime.h>
#include <stdint.h>

typedef __bf16 bf16x2 __attribute__((ext_vector_type(2)));
typedef __bf16 bf16x4 __attribute__((ext_vector_type(4)));
typedef __bf16 bf16x8 __attribute__((ext_vector_type(8)));
typedef short s16x4 __attribute__((ext_vector_type(4)));
typedef float f32x2 __attribute__((ext_vector_type(2)));
typedef float f32x4 __attribute__((ext_vector_type(4)));
typedef float f32x16 __attribute__((ext_vector_type(16)));
typedef unsigned u32x2 __attribute__((ext_vector_type(2)));
typedef unsigned u32x4 __attribute__((ext_vector_type(4)));
typedef int i32x4 __attribute__((ext_vector_type(4)));
#define LDS_AS __attribute__((address_space(3)))
#define DI __device__ __forceinline__

constexpr int S_ = 2048, D_ = 640, DF_ = 1728;
constexpr unsigned GRP = 74 * 4096;
constexpr unsigned W2T = 10u << 20;
constexpr unsigned CTL = 16u << 20;
constexpr unsigned GRN = CTL + 32768;
constexpr unsigned RDY = CTL + 65536;
constexpr unsigned PRG = CTL + 98304;
constexpr unsigned WFL = CTL + 131072;
constexpr unsigned AFL = RDY + 160 * 128 + 32;
constexpr unsigned CSP = CTL + 262144;
constexpr unsigned WS_BYTES = 32u << 20;
constexpr unsigned OOB = 0x70000000u;
constexpr int wslot(int s) { const int a[5] = {0, 40960, 57344, 98304, 114688}; return a[s % 5]; }
constexpr int xslot(int c) { const int a[3] = {131072, 16384, 73728}; return a[c % 3]; }
constexpr int L_Q = 40960, QP = 496, PIECE = 40960, L_G = 155648, L_CS = 162816;
constexpr int LDS_BYTES = 163840;
#define SPIN_CAP (1 << 17)

DI i32x4 mk_srd(const void* p, unsigned bytes) {
  uint64_t a = (uint64_t)p;
  i32x4 r;
  r[0] = (int)(unsigned)a; r[1] = (int)(unsigned)(a >> 32); r[2] = (int)bytes; r[3] = 0x00020000;
  return r;
}
#define PIN(x) asm volatile("" : "+v"(x))
#define SPIN(x) asm volatile("" : "+s"(x))
#define SB() __builtin_amdgcn_sched_barrier(0)
#define MB() asm volatile("" ::: "memory")
#define WAIT_L0() do { MB(); __builtin_amdgcn_s_waitcnt(0xC07F); MB(); } while (0)
#define BAR() do { __builtin_amdgcn_s_barrier(); MB(); } while (0)
#define RFL(x) __builtin_amdgcn_readfirstlane(x)
#define TS(i)
#define TSR(i)
#define TSDECL
#define TSOUT()
#define PT(i)
#define ET(i)

DI bf16x8 lds_b128(unsigned a) { return *(LDS_AS bf16x8*)(a); }
DI u32x4 lds_u4(unsigned a) { return *(LDS_AS u32x4*)(a); }
DI f32x4 lds_f4(unsigned a) { return *(LDS_AS f32x4*)(a); }
DI f32x2 lds_f2(unsigned a) { return *(LDS_AS f32x2*)(a); }
DI bf16x4 lds_tr1(unsigned a) { return __builtin_amdgcn_ds_read_tr16_b64_v4bf16((LDS_AS bf16x4*)(a)); }
DI bf16x8 cat8(bf16x4 lo, bf16x4 hi) { return __builtin_shufflevector(lo, hi, 0, 1, 2, 3, 4, 5, 6, 7); }
DI f32x4 mfma(bf16x8 a, bf16x8 b, f32x4 c) { return __builtin_amdgcn_mfma_f32_16x16x32_bf16(a, b, c, 0, 0, 0); }
DI f32x4 mfma4(u32x2 a, u32x2 b, f32x4 c) { return __builtin_amdgcn_mfma_f32_4x4x4bf16_1k(__builtin_bit_cast(s16x4, a), __builtin_bit_cast(s16x4, b), c, 0, 0, 0); }
DI void mac32(f32x16& c, bf16x8 a, bf16x8 b, bool first) {
  if (first) asm volatile("v_mfma_f32_32x32x16_bf16 %0, %1, %2, 0" : "=v"(c) : "v"(a), "v"(b));
  else asm volatile("v_mfma_f32_32x32x16_bf16 %0, %1, %2, %0" : "+v"(c) : "v"(a), "v"(b));
}
DI void mac16(f32x4& c, u32x4 a, u32x4 b, bool first) {
  if (first) asm volatile("v_mfma_f32_16x16x32_bf16 %0, %1, %2, 0" : "=v"(c) : "v"(a), "v"(b));
  else asm volatile("v_mfma_f32_16x16x32_bf16 %0, %1, %2, %0" : "+v"(c) : "v"(a), "v"(b));
}
DI unsigned pk2(f32x2 f) { return __builtin_bit_cast(unsigned, __builtin_convertvector(f, bf16x2)); }
DI unsigned pk(float a, float b) { return pk2(f32x2{a, b}); }
DI unsigned nofill(unsigned v) { return min(v, 0xfffffffeu); }
DI bf16x8 as8(u32x4 v) { return __builtin_bit_cast(bf16x8, v); }
DI f32x2 sp2(float a) { return f32x2{a, a}; }
DI f32x2 fma2(f32x2 a, f32x2 b, f32x2 c) { return __builtin_elementwise_fma(a, b, c); }
DI f32x2 sw16(float a, float b) { return __builtin_bit_cast(f32x2, __builtin_amdgcn_permlane16_swap(__float_as_uint(a), __float_as_uint(b), false, false)); }
DI f32x2 sw32(float a, float b) { return __builtin_bit_cast(f32x2, __builtin_amdgcn_permlane32_swap(__float_as_uint(a), __float_as_uint(b), false, false)); }
template <int C> DI float dpp_add(float v) { return v + __uint_as_float(__builtin_amdgcn_update_dpp(0u, __float_as_uint(v), C, 15, 15, true)); }
DI float wave_sum(float v) {
  v = dpp_add<0xB1>(v); v = dpp_add<0x4E>(v); v = dpp_add<0x141>(v); v = dpp_add<0x140>(v);
  f32x2 r = sw16(v, v); v = r[0] + r[1];
  r = sw32(v, v); return r[0] + r[1];
}
DI float blo(unsigned d) { return __uint_as_float(d << 16); }
DI float bhi(unsigned d) { return __uint_as_float(d & 0xffff0000u); }
constexpr unsigned ONES = 0x3f803f80u;

DI unsigned ld1c(i32x4 srd, unsigned vo, unsigned so) {
  unsigned r, t;
  asm volatile("s_mov_b32 %1, %4\n\tbuffer_load_dword %0, %2, %3, %1 offen sc1" : "=v"(r), "=&s"(t) : "v"(vo), "s"(srd), "s"(RFL(so)) : "memory");
  return r;
}
DI u32x4 ld4c(i32x4 srd, unsigned vo, unsigned so) {
  u32x4 r; unsigned t;
  asm volatile("s_mov_b32 %1, %4\n\tbuffer_load_dwordx4 %0, %2, %3, %1 offen sc1" : "=v"(r), "=&s"(t) : "v"(vo), "s"(srd), "s"(RFL(so)) : "memory");
  return r;
}
DI u32x4 ld4o(i32x4 srd, unsigned vo, unsigned so, int off) {
  u32x4 r; unsigned t;
  if (off < 4096) asm volatile("buffer_load_dwordx4 %0, %1, %2, %3 offen offset:%4 sc1" : "=v"(r) : "v"(vo), "s"(srd), "s"(so), "i"(off) : "memory");
  else asm volatile("s_add_u32 %1, %4, %5\n\tbuffer_load_dwordx4 %0, %2, %3, %1 offen sc1" : "=v"(r), "=&s"(t) : "v"(vo), "s"(srd), "s"(so), "i"(off) : "memory", "scc");
  return r;
}
DI void st4o(u32x4 d, i32x4 srd, unsigned vo, unsigned so) {
  asm volatile("buffer_store_dwordx4 %0, %1, %2, %3 offen sc1\n\ts_nop 1" :: "v"(d), "v"(vo), "s"(srd), "s"(so) : "memory");
}
#define CLM "sc1"
#define HSM "sc1"
DI void st4f(u32x4 d, i32x4 srd, unsigned vo, unsigned so) {
  asm volatile("buffer_store_dwordx4 %0, %1, %2, %3 offen " CLM "\n\ts_nop 1" :: "v"(d), "v"(vo), "s"(srd), "s"(so) : "memory");
}
DI void st4h(u32x4 d, i32x4 srd, unsigned vo, unsigned so) {
  asm volatile("buffer_store_dwordx4 %0, %1, %2, %3 offen " HSM "\n\ts_nop 1" :: "v"(d), "v"(vo), "s"(srd), "s"(so) : "memory");
}
DI void st2h(u32x2 d, i32x4 srd, unsigned vo, unsigned so) {
  asm volatile("buffer_store_dwordx2 %0, %1, %2, %3 offen sc1\n\ts_nop 0" :: "v"(d), "v"(vo), "s"(srd), "s"(so) : "memory");
}
DI u32x2 lds_u2(unsigned a) { return *(LDS_AS u32x2*)(a); }
DI void ld2x2c(u32x2& a, u32x2& b, i32x4 srd, unsigned vo, unsigned so, int k) {
  unsigned t;
  asm volatile("s_add_u32 %2, %5, %6\n\tbuffer_load_dwordx2 %0, %3, %4, %2 offen sc1\n\tbuffer_load_dwordx2 %1, %3, %4, %2 offen offset:2048 sc1"
               : "=&v"(a), "=&v"(b), "=&s"(t) : "v"(vo), "s"(srd), "s"(RFL(so)), "i"(k) : "memory", "scc");
}
DI void ldk(u32x4 (&a)[4], i32x4 srd, unsigned vo, unsigned so) {
  unsigned t;
  asm volatile("s_mov_b32 %4, %7\n\tbuffer_load_dwordx4 %0, %5, %6, %4 offen sc1\n\tbuffer_load_dwordx4 %1, %5, %6, %4 offen offset:1024 sc1\n\t"
               "buffer_load_dwordx4 %2, %5, %6, %4 offen offset:2048 sc1\n\tbuffer_load_dwordx4 %3, %5, %6, %4 offen offset:3072 sc1"
               : "=&v"(a[0]), "=&v"(a[1]), "=&v"(a[2]), "=&v"(a[3]), "=&s"(t) : "v"(vo), "s"(srd), "s"(RFL(so)) : "memory");
}
DI void ldk_again(u32x4 (&a)[4], i32x4 srd, unsigned vo, unsigned so) {
  unsigned t;
  asm volatile("s_mov_b32 %4, %7\n\tbuffer_load_dwordx4 %0, %5, %6, %4 offen sc1\n\tbuffer_load_dwordx4 %1, %5, %6, %4 offen offset:1024 sc1\n\t"
               "buffer_load_dwordx4 %2, %5, %6, %4 offen offset:2048 sc1\n\tbuffer_load_dwordx4 %3, %5, %6, %4 offen offset:3072 sc1\n\ts_waitcnt vmcnt(0)"
               : "+v"(a[0]), "+v"(a[1]), "+v"(a[2]), "+v"(a[3]), "=&s"(t) : "v"(vo), "s"(srd), "s"(RFL(so)) : "memory");
}
DI void ldk(u32x4 (&a)[5], i32x4 srd, unsigned vo, unsigned so) {
  unsigned t, u;
  asm volatile("s_mov_b32 %5, %9\n\tbuffer_load_dwordx4 %0, %7, %8, %5 offen sc1\n\tbuffer_load_dwordx4 %1, %7, %8, %5 offen offset:1024 sc1\n\t"
               "s_add_u32 %6, %9, 0x1000\n\t"
               "buffer_load_dwordx4 %2, %7, %8, %5 offen offset:2048 sc1\n\tbuffer_load_dwordx4 %3, %7, %8, %5 offen offset:3072 sc1\n\t"
               "buffer_load_dwordx4 %4, %7, %8, %6 offen sc1"
               : "=&v"(a[0]), "=&v"(a[1]), "=&v"(a[2]), "=&v"(a[3]), "=&v"(a[4]), "=&s"(t), "=&s"(u) : "v"(vo), "s"(srd), "s"(RFL(so)) : "memory", "scc");
}
DI void ldrows(unsigned (&d)[8], i32x4 srd, unsigned vo, unsigned so) {
  unsigned t, u;
  asm volatile("s_mov_b32 %8, %12\n\ts_add_u32 %9, %12, 0x1400\n\t"
               "buffer_load_dword %0, %10, %11, %8 offen\n\tbuffer_load_dword %1, %10, %11, %8 offen offset:1280\n\t"
               "buffer_load_dword %2, %10, %11, %8 offen offset:2560\n\tbuffer_load_dword %3, %10, %11, %8 offen offset:3840\n\t"
               "buffer_load_dword %4, %10, %11, %9 offen\n\tbuffer_load_dword %5, %10, %11, %9 offen offset:1280\n\t"
               "buffer_load_dword %6, %10, %11, %9 offen offset:2560\n\tbuffer_load_dword %7, %10, %11, %9 offen offset:3840"
               : "=&v"(d[0]), "=&v"(d[1]), "=&v"(d[2]), "=&v"(d[3]), "=&v"(d[4]), "=&v"(d[5]), "=&v"(d[6]), "=&v"(d[7]), "=&s"(t), "=&s"(u)
               : "v"(vo), "s"(srd), "s"(RFL(so)) : "memory", "scc");
}
DI void st4c(u32x4 d, i32x4 srd, unsigned vo, unsigned so, int off) {
  unsigned t;
  asm volatile("s_mov_b32 %0, %4\n\tbuffer_store_dwordx4 %1, %2, %3, %0 offen offset:%5 sc1\n\ts_nop 1" : "=&s"(t) : "v"(d), "v"(vo), "s"(srd), "s"(RFL(so)), "i"(off) : "memory");
}
DI void st1c(unsigned d, i32x4 srd, unsigned vo, unsigned so) {
  unsigned t;
  asm volatile("s_mov_b32 %0, %4\n\tbuffer_store_dword %1, %2, %3, %0 offen sc1" : "=&s"(t) : "v"(d), "v"(vo), "s"(srd), "s"(RFL(so)) : "memory");
}
DI void st4l(u32x4 d, i32x4 srd, unsigned so) {
  unsigned t;
  asm volatile("s_mov_b32 %0, %3\n\ts_mov_b64 exec, 1\n\tbuffer_store_dwordx4 %1, off, %2, %0 sc1\n\ts_mov_b64 exec, -1\n\ts_nop 0"
               : "=&s"(t) : "v"(d), "s"(srd), "s"(RFL(so)) : "memory");
}
DI void st1l(unsigned d, i32x4 srd, unsigned so) {
  unsigned t;
  asm volatile("s_mov_b32 %0, %3\n\ts_mov_b64 exec, 1\n\tbuffer_store_dword %1, off, %2, %0 sc1\n\ts_mov_b64 exec, -1"
               : "=&s"(t) : "v"(d), "s"(srd), "s"(RFL(so)) : "memory");
}
DI int cap(int n) { return n > 63 ? 63 : n; }
DI void wv(int n) { asm volatile("s_waitcnt vmcnt(%0)" :: "i"(cap(n)) : "memory"); }
DI void wvl(int n) { asm volatile("s_waitcnt vmcnt(%0)" :: "i"(cap(n)) : "memory"); __builtin_amdgcn_s_waitcnt(0xC07F); MB(); }
template <class T> DI void got(int n, T& a) { asm volatile("s_waitcnt vmcnt(%1)" : "+v"(a) : "i"(cap(n)) : "memory"); }
template <class T, class U> DI void got(int n, T& a, U& b) { asm volatile("s_waitcnt vmcnt(%2)" : "+v"(a), "+v"(b) : "i"(cap(n)) : "memory"); }
template <class T, int N> DI void gotk(int n, T (&a)[N]) {
  asm volatile("s_waitcnt vmcnt(%0)" :: "i"(cap(n)) : "memory");
#pragma unroll
  for (int i = 0; i < N; ++i) PIN(a[i]);
}
DI int lane_id() {
  int l;
  asm volatile("v_mbcnt_lo_u32_b32 %0, -1, 0\n\tv_mbcnt_hi_u32_b32 %0, -1, %0" : "=v"(l));
  return l;
}
DI void PINV(u32x4& v) { PIN(v); }
DI unsigned opq(unsigned v) { PIN(v); return v; }
DI unsigned addk(unsigned v, int k) { unsigned r; asm volatile("v_add_u32 %0, %2, %1" : "=v"(r) : "v"(v), "i"(k)); return r; }
DI unsigned xadk(unsigned v, int k) { unsigned r; asm volatile("v_xad_u32 %0, %1, 32, %2" : "=v"(r) : "v"(v), "s"(k)); return r; }

DI void dma2(i32x4 srd, unsigned v0, unsigned v1, unsigned sbase, int soff, unsigned lbase, int loff) {
  unsigned t;
  asm volatile("s_add_u32 m0, %4, %5\n\ts_add_u32 %0, %7, %6\n\tbuffer_load_dwordx4 %1, %3, %0 offen lds\n\t"
               "buffer_load_dwordx4 %2, %3, %0 offen offset:1024 lds"
               : "=&s"(t) : "v"(v0), "v"(v1), "s"(srd), "s"(lbase), "i"(loff), "i"(soff), "s"(sbase) : "memory", "scc");
}
DI void dma1c(i32x4 srd, unsigned v2, unsigned sbase, int soff, unsigned lbase, int loff) {
  unsigned t;
  asm volatile("s_add_u32 m0, %3, %4\n\ts_add_u32 %0, %6, %5\n\tbuffer_load_dwordx4 %1, %2, %0 offen offset:2048 lds"
               : "=&s"(t) : "v"(v2), "s"(srd), "s"(lbase), "i"(loff), "i"(soff), "s"(sbase) : "memory", "scc");
}
DI void dma5(i32x4 srd, unsigned v, unsigned so, unsigned lbase, int loff) {
  unsigned t, u;
  asm volatile("s_add_u32 m0, %5, %6\n\ts_mov_b32 %0, %4\n\tbuffer_load_dwordx4 %2, %3, %0 offen sc1 lds\n\t"
               "buffer_load_dwordx4 %2, %3, %0 offen offset:1024 sc1 lds\n\ts_add_u32 %1, %4, 0x400\n\t"
               "buffer_load_dwordx4 %2, %3, %0 offen offset:2048 sc1 lds\n\t"
               "buffer_load_dwordx4 %2, %3, %0 offen offset:3072 sc1 lds\n\t"
               "s_add_u32 m0, %5, %7\n\ts_nop 0\n\t"
               "buffer_load_dwordx4 %2, %3, %1 offen offset:3072 sc1 lds"
               : "=&s"(t), "=&s"(u) : "v"(v), "s"(srd), "s"(RFL(so)), "s"(lbase), "i"(loff), "i"(loff + 1024) : "memory", "scc");
}
DI void dma1s(i32x4 srd, unsigned v, unsigned so, unsigned lbase, int loff) {
  unsigned t;
  asm volatile("s_add_u32 m0, %4, %5\n\ts_mov_b32 %0, %3\n\tbuffer_load_dwordx4 %1, %2, %0 offen sc1 lds"
               : "=&s"(t) : "v"(v), "s"(srd), "i"(so), "s"(lbase), "i"(loff) : "memory", "scc");
}
DI void dmap(i32x4 srd, unsigned v, unsigned so, int loff) {
  unsigned t;
  asm volatile("s_mov_b32 m0, %4\n\ts_mov_b32 %0, %3\n\tbuffer_load_dword %1, %2, %0 offen sc1 lds"
               : "=&s"(t) : "v"(v), "s"(srd), "s"(RFL(so)), "i"(loff) : "memory");
}
template <int N> struct IC { static constexpr int v = N; };
template <int A, int B, class F> DI void sfor(F&& f) { if constexpr (A < B) { f(IC<A>{}); sfor<A + 1, B>(f); } }

struct P {
  const void *x, *wqkv, *wo, *wg, *wu, *wd;
  void* out;
  char* ws;
};

#define ER 0
struct Op { int kind, idx; };
constexpr Op ring(int s, int k) {
  if (s == -2 || s == -3) return Op{0, 0};
  if (s <= 0) { const int c = s == -4 ? 0 : s + 2; const Op o[4] = {{1, 2 * c}, {2, c}, {3, c}, {1, 2 * c + 1}}; return o[k]; }
  if (s > 14 || k > 1) return Op{0, 0};
  if (k == 0) return Op{1, s + 5};
  return (s & 1) ? Op{2, (s + 5) / 2} : Op{3, (s + 4) / 2};
}
constexpr unsigned long long sigma_tab(int j) {
  unsigned long long t = 0; int e = 0;
  for (int c = 0; c < 3; ++c)
    for (int s = (j + 3 - c) % 3; s < 13; s += 3) t |= (unsigned long long)s << (4 * e++);
  return t;
}
#define NCS 1
#define OPT 6
#define AFG 1
#define AFE 1
#define IWR 8
#define EAA 0
#define EAM0 0
#define EAM1 0
#define EAP 0
#define EMT0 2
#define EMT1 1
#define AXF 1
#define AXP 1
#define L48 0
#define RYA 2
#define WFD 2
#define NOCL 0
#define CLS 1
#define ADM 5
#define ARE 2
#define RD5 2
#define PR2 0
#define PRP 0
#define AQO 0
#define AB2 0
#define W64 1
#define HS 0
#define DQ 0
#define PTC 0
#define T2T 0
#define ZP 2
#define SNK 0
#define ADI 1
#define ANB 0
#define RYL -1
#define CSS 10
#define CSL 14
#define CST 18
#define HO 7
#define IPF 1
#define IPK 64
#define IPX 0
#define IPA 320
#define IPA2 320
#define IXA 5
#define IPB -1
#define IPB2 1
#define TSA 5
#define TSD 4
#define SL -2
#define TL -2
#define TD 3
#define GL 8
#define CPF 4
#define HPF 0
#define FSP 19
#define CKA 13
#define AD 3
#define BP0 0
#define PSL 4
#define SWF 9
#define CL0 6
#define SPF 0
#define LFE 2
#define TSL 40
#define DEP0 3
#define NRE 3

#define PRF 0
#define PRS 2
#define MPR 1
#define TLW 99
#define TLS 16
#define RB 152
#define RBA RB
#define LFW 5
constexpr int KP = DQ ? 336 : QP, VP = DQ ? 176 : QP, L_V = DQ ? L_Q + 192 * KP : L_Q + 320;
constexpr int piece(bool att, int i) { return att ? (i == NRE ? 0 : (i >= NRE + 3 + AXF && i < NRE + 6) ? i - NRE - 2 : -1) : ((i >= NRE && i < NRE + 4) ? i - NRE : -1); }
constexpr int f_when(bool att, int i) { return i >= (att ? NRE + 6 : NRE + 4 + LFE) ? i - 9 + LFW : 0; }
constexpr int a_upto(bool att, int ns, int i) {
  int lim = ns < i + AD ? ns : i + AD;
  for (int k = ns - 1; k >= i; --k) {
    int f = 20;
    for (int j = k + 1; j < ns; ++j) if (piece(att, j) < 0 && f_when(att, j) <= k) f += 20;
    const int u = k + ((att ? RBA : RB) - f) / 16;
    if (u < lim) lim = u;
  }
  return lim;
}

template <int TYPE, int ROLE>
DI void body(const P& p, int job, int wave, i32x4 sc0) {
  constexpr bool ATT = TYPE == 1;
  TSDECL; TSR(0); TS(1);
  int vm = 0;
  int idH = 0, idG = 0, idW[20], idX[10], idT = 0, idF[10], idA[10], idC = 0, idP = 0, idR = 0;
  const int b = blockIdx.x, xcd = b & 7, bi = b >> 3, wr = wave & 3;
  const i32x4 sa_ = mk_srd(p.ws, WS_BYTES);
  const i32x4 sx = mk_srd((const char*)p.x - 2048, S_ * D_ * 2 + 2048);

  const int sidx = (xcd >> 2) * 1024 + (xcd & 3) * 256 + bi * 8 + wave;
  unsigned ev, sc_;
  u32x4 sa;
  int idS = 0;
  asm volatile("buffer_load_dword %0, off, %1, %2 sc1" : "=&v"(ev) : "s"(sa_), "s"(CTL) : "memory");
  vm += 1;
  auto st_load = [&]() {
    const int lane = lane_id(), stok = 2 * (sidx & 1023) + (sidx >> 10);
    asm volatile("buffer_load_dwordx4 %0, %2, %4, %5 offen offset:2048\n\tbuffer_load_dword %1, %3, %4, %5 offen offset:3072"
                 : "=&v"(sa), "=&v"(sc_) : "v"(lane * 16), "v"(lane * 4), "s"(sx), "s"(stok * 1280) : "memory");
    vm += 2; idS = vm;
  };
  if (TYPE == 2 || SL < -1) st_load();
  SB();
  {
    const int l = lane_id();
    dmap(sc0, l < CPF ? l * 128 : OOB, (bi * 8 + wave) * (CPF * 128), L_G + 4096);
    vm += 1;
  }
  SB();

  PT(4);
  int pn = 0, slice = 0, G = 0, th = 0, cj = 5;
  if (ATT) { G = job & 7; pn = job >> 3; th = pn >= 6; pn -= 6 * th; cj = pn; }
  else { pn = job / 14; slice = job - pn * 14; th = pn > 5 || (pn == 5 && slice >= 7); cj = pn - 6 * (pn > 5); if (pn == 5) cj = 5; }
  const int sl3 = slice % 3;
  auto t32_of = [&](int q) {
    int j = (q >> 1) + sl3; j -= j >= 3 ? 3 : 0;
    return 6 * pn + 2 * j + (q & 1);
  };
  auto tau_of = [&](int q, int r) {
    return ATT ? th * 1024 + min(max(176 * pn - 16 + 32 * q + r, 0), 1023) : min(t32_of(q), 63) * 32 + r;
  };

  unsigned xo[3], wo_[2];
  i32x4 sw;
  constexpr int wrow = ATT ? 3840 : 3456;
  const unsigned wb0 = th * (320 * wrow), wb1 = -wb0, xb0 = th * 640, xb1 = -xb0;
  const unsigned wdst = wave * 2048, xdst = wave * 3072;
  if (TYPE != 2) {
    const int lane = lane_id();
    if (ATT) {
      sw = mk_srd(p.wqkv, D_ * 1920 * 2);
#pragma unroll
      for (int i = 0; i < 2; ++i) {
        const int row = (wave * 2 + i) * 2 + (lane >> 5), c = min((lane & 31) ^ ((row & 3) << 2), 29);
        const int m = (c * 26) >> 8;
        wo_[i] = row * wrow + m * (1280 - 160) + G * 160 + c * 16 - i * 1024;
        if (ZP && ((lane & 31) ^ ((row & 3) << 2)) > 29) wo_[i] = OOB;
      }
    } else {
      sw = mk_srd(ROLE ? p.wu : p.wg, D_ * DF_ * 2);
      const int c = ((lane & 15) ^ ((lane >> 4) << 2)) & (slice == 13 ? 7 : 15);
#pragma unroll
      for (int i = 0; i < 2; ++i) wo_[i] = ((wr * 2 + i) * 4 + (lane >> 4)) * wrow + slice * 256 + c * 16 - i * 1024;
      if (ZP && slice == 13 && (((lane & 15) ^ ((lane >> 4) << 2)) & 8)) wo_[0] = wo_[1] = OOB;
    }
#pragma unroll
    for (int i = 0; i < 3; ++i) {
      const int pc = wave * 3 + i, r = (pc & 3) * 8 + (lane >> 3), tau = tau_of(pc >> 2, r);
      xo[i] = (2 * (tau & 1023) + (tau >> 10)) * (D_ * 2) + (((lane & 7) ^ ((r >> 1) & 7)) << 4) + 2048 - i * 1024;
      if (ZP && (ATT ? (ZP > 1 && pc == 0) || 176 * pn - 16 + 32 * (pc >> 2) + r > 1023 : t32_of(pc >> 2) > 63)) xo[i] = OOB;
    }
  }
  auto ring_ops = [&](auto sc) {
    constexpr int s = decltype(sc)::v;
    sfor<0, 4>([&](auto kc) {
      constexpr Op o = ring(s, decltype(kc)::v);
      if constexpr (o.kind == 1) { dma2(sw, wo_[0], wo_[1], o.idx < 10 ? wb0 : wb1, o.idx * 32 * wrow, wdst, wslot(o.idx)); vm += 2; idW[o.idx] = vm; }
      if constexpr (o.kind == 2) { dma2(sx, xo[0], xo[1], o.idx < 5 ? xb0 : xb1, o.idx * 128, xdst, xslot(o.idx)); vm += 2; }
      if constexpr (o.kind == 3) { dma1c(sx, xo[2], o.idx < 5 ? xb0 : xb1, o.idx * 128, xdst, xslot(o.idx)); vm += 1; idX[o.idx] = vm; }
    });
  };
  unsigned tr_[8];
  auto tu_load = [&](int u, unsigned (&tr_)[8]) {
    const int l = lane_id(), kb = u / 20, rem = u - kb * 20, hf = rem >= 10, cb = rem - 10 * hf;
    const i32x4 sd = mk_srd(kb < 54 ? p.wd : p.wo, kb < 54 ? DF_ * D_ * 2 : D_ * D_ * 2);
    ldrows(tr_, sd, (l >> 5) * (8 * 1280) + (l & 31) * 4, ((kb < 54 ? kb : kb - 54) * 32 + hf * 16) * 1280 + cb * 128);
    vm += 8; idT = vm;
  };
  auto tu_store = [&](int u, unsigned (&tr_)[8], int left) {
    const int l = lane_id(), kb = u / 20, rem = u - kb * 20, hf = rem >= 10, cb = rem - 10 * hf;
    gotk(left, tr_);
    u32x4 e, o;
#pragma unroll
    for (int j = 0; j < 4; ++j) {
      e[j] = __builtin_amdgcn_perm(tr_[2 * j + 1], tr_[2 * j], 0x05040100u);
      o[j] = __builtin_amdgcn_perm(tr_[2 * j + 1], tr_[2 * j], 0x07060302u);
    }
    const unsigned vo = ((l >> 3) & 3) * 1024 + (l >> 5) * 256 + (l & 7) * 16, so = kb < 74 ? W2T + kb * 40960 + cb * 4096 + hf * 512 : OOB;
    st4c(e, sa_, vo, so, 0); st4c(o, sa_, vo, so, 128);
    vm += 2; idT = vm;
  };
  unsigned pfo[3], pfx[3];
  auto ipf_prep = [&]() {
    const int l = lane_id();
    if (TYPE != 2) {
      int row; unsigned col;
      if (ATT) { const int n = l * 96 + bi * 8 + wave, q = n % 9; row = n / 9; col = (q / 3) * 1280 + (((xcd & 3) * 320) & ~127) + (q % 3) * 128; }
      else { row = cj < 5 ? 5 * ((l >> 1) * 4 + wr) + cj : 999; col = slice * 256 + (slice == 13 ? 0 : (l & 1) * 128); }
      const unsigned o = row * wrow + col;
      pfo[0] = opq(row >= IPK && row < IPA ? o : OOB);
      pfo[1] = opq(row >= IPA && row < IPA2 ? o : OOB);
      pfo[2] = opq(row >= IPA2 && row < 320 ? o : OOB);
    }
    if (IPX) {
      const int t = (bi * 8 + wave) * 4 + (l & 3), c = l >> 2;
      const unsigned o = (2 * t + (xcd >> 2)) * 1280 + c * 128 + 2048;
      pfx[0] = opq(c >= 1 && c < IXA ? o : OOB);
      pfx[1] = opq(c >= IXA && c < 5 ? o : OOB);
    }
  };
  auto ipf = [&](int k) {
    if (k == 0) ipf_prep();
    if (TYPE != 2 && (k == 0 || IPA < 320) && (k < 2 || IPA2 < 320)) { dmap(sw, pfo[k], wb0, L_G + 4096); vm += 1; }
    if (IPX && k < 2 && (k == 0 || IXA < 5)) { dmap(sx, pfx[k], (xcd >> 2) * 640, L_G + 4096); vm += 1; }
    SB();
  };
  PT(5);
  if (IPF == 0) ipf(0);
  if (TYPE != 2) ring_ops(IC<-4>{});
  if (IPF == 1 || (IPF == 3 && ROLE == 1) || (IPF >= 2 && IPF < 9 && TYPE == 2)) ipf(0);
  PT(6);
  auto rest = [&]() {
    SB();
    if (TYPE == 0 && TL == -3) tu_load(job * 8 + wave, tr_);
    if (TYPE != 2) { ring_ops(IC<-3>{}); ring_ops(IC<-2>{}); }
    if (TYPE == 0 && TL == -2) tu_load(job * 8 + wave, tr_);
    SB();
  };
  if (!((HO >> ROLE) & 1)) rest();
  auto touch = [&]() {
    const int l = lane_id();
    if (ROLE) dmap(mk_srd(p.out, S_ * D_ * 2), wr == 0 ? l << TLS : OOB, 0, L_G + 4096);
    else dmap(sa_, (wr == 0 && l < 52) ? l << 18 : OOB, 0, L_G + 4096);
    vm += 1;
  };
  if (TLW == -2) touch();
  SB();

  unsigned ep, tag;
  auto st_take = [&]() {
    got(vm - idS, sa, sc_);
    float e[10] = {blo(sa[0]), bhi(sa[0]), blo(sa[1]), bhi(sa[1]), blo(sa[2]), bhi(sa[2]), blo(sa[3]), bhi(sa[3]), blo(sc_), bhi(sc_)};
    float s = 0;
#pragma unroll
    for (int i = 0; i < 10; ++i) s += e[i];
    float nm = wave_sum(s) * (-1.0f / D_), q = 0;
#pragma unroll
    for (int i = 0; i < 10; ++i) { float d = e[i] + nm; q = fmaf(d, d, q); }
    float rs = __builtin_amdgcn_rsqf(wave_sum(q) * (1.0f / D_) + 1e-5f);
    st4l(u32x4{__float_as_uint(rs), tag, __float_as_uint(nm), tag}, sa_, GRN + sidx * 16);
    vm += 1;
  };
  auto first = [&]() {
    SB();
    got(vm - 1, ev);
    ep = RFL(ev); tag = ep * 0x9E3779B1u + 0x7F4A7C15u;
    if (TYPE == 2 || SL < -1) st_take();
    SB();
    TS(2);
  };
  if (TYPE == 2) first();

  const int grp = xcd * 4 + (bi >> 3), ct = bi & 7, gj = grp % 3;
  const unsigned long long tab = gj == 0 ? sigma_tab(0) : gj == 1 ? sigma_tab(1) : sigma_tab(2);
  constexpr int NS = ROLE ? 10 : 9, OP = ROLE ? OPT : -1, NA = (ROLE && OPT == 9) ? 6 : 7;
  auto kb_of = [&](int i) {
    return i == OP ? 52 + (wr & 1) : i < NA ? 4 * (int)((tab >> (8 * i + 4 * ROLE)) & 15) + wr : 54 + 8 * ROLE + (2 + ROLE) * wr + i - NA;
  };
  u32x4 F[10][5];
  const unsigned pdst = wave * 5120;
  const unsigned l16 = opq(lane_id() * 16);
  unsigned fso[10];
#pragma unroll
  for (int i = 0; i < NS; ++i) { fso[i] = (i == OP && wr > 1 && piece(ATT, i) < 0) ? OOB : W2T + kb_of(i) * 40960 + ct * 5120; if (TYPE != 2) SPIN(fso[i]); }
  auto ldF = [&](int i) { ldk(F[i], sa_, l16, fso[i]); vm += 5; idF[i] = vm; };
  auto dmaF = [&](int i, int pc) {
    dma5(sa_, l16, fso[i], pdst, pc * PIECE); vm += 5; idF[i] = vm; };
  auto rdF = [&](int i, int pc) {
    const unsigned a = opq(lane_id() * 16 + pdst + pc * PIECE);
#pragma unroll
    for (int n = 0; n < 5; ++n) F[i][n] = lds_u4(a + n * 1024);
  };
  unsigned wfo, wfv;
  {
    const int l = lane_id();
    wfo = opq(kb_of(min(l >> 2, NS - 1)) * 128 + ((5 * ct) >> 2) * 8 + (l & 3) * 4);
  }
  auto wf_issue = [&]() { wfv = ld1c(sa_, wfo, WFL); vm += 1; idP = vm; };
  auto wf_check = [&]() {
    got(vm - idP, wfv);
#pragma nounroll
    for (int it = 0; it < SPIN_CAP && !__all(wfv == tag); ++it) {
      __builtin_amdgcn_s_sleep(2);
      wfv = ld1c(sa_, wfo, WFL);
      got(0, wfv);
    }
  };
  unsigned ryv, ryo;
  {
    const int l = lane_id(), t16 = 4 * (grp & 15) + 3 * ((l >> 3) & 1);
    ryo = opq((!AFG && (l & 16) ? 160 + ((grp >> 4) * 6 + t16 / 11) * 8 + (l & 7) : (grp / 3) * 14 + min(l & 15, 13)) * 128);
  }
  auto ry_load = [&]() { ryv = ld1c(sa_, ryo, RDY); };
  auto ry_issue = [&]() { ry_load(); vm += 1; idR = vm; };
  auto ry_check = [&]() {
    got(vm - idR, ryv);
#pragma nounroll
    for (int it = 0; it < SPIN_CAP && !__all(ryv == tag); ++it) {
      __builtin_amdgcn_s_sleep(8);
      ry_load();
      got(0, ryv);
    }
  };
  u32x4 A[10][4];
  auto a_so = [&](int i) { return (i == OP && wr > 1) ? OOB : kb_of(i) * 4096 + grp * GRP; };
  auto ldA = [&](int i) { ldk(A[i], sa_, l16, a_so(i)); vm += 4; idA[i] = vm; };
  constexpr int EA = TYPE == 2 ? 0 : ATT ? EAA : ROLE ? EAM1 : EAM0;
  auto early = [&]() {
    if (EA) ry_check();
    sfor<0, EA>([&](auto ic) { ldA(decltype(ic)::v); });
    SB();
  };
  auto tu_flag = [&](int u) {
    const int kb = u / 20, rem = u - kb * 20, hf = rem >= 10, cb = rem - 10 * hf;
    wv(vm - idT);
    st1l(tag, sa_, kb < 74 ? WFL + kb * 128 + cb * 8 + hf * 4 : OOB);
    vm += 1;
  };

  if constexpr (TYPE == 2) {
    unsigned t6[6][8];
    const int u0 = 1232 + job * 8 + wave;
#pragma unroll
    for (int k = 0; k < 6; ++k) tu_load(u0 + 48 * k, t6[k]);
#pragma unroll
    for (int k = 0; k < 6; ++k) tu_store(u0 + 48 * k, t6[k], 40 - 6 * k);
#pragma unroll
    for (int k = 0; k < 6; ++k) tu_flag(u0 + 48 * k);
    BAR();
    st1l(tag, sa_, wave == 0 ? RDY + (154 + job) * 128 : OOB);
    vm += 1;
    if (T2T == 2) __builtin_amdgcn_s_sleep(100);
#pragma unroll
    for (int r = 0; r < 7 * !!T2T; ++r) {
      const int q = job * 8 + wave + 48 * r, g = q / 10;
      dmap(sa_, l16 * 8, q < 320 ? g * GRP + 54 * 4096 + (q - 10 * g) * 8192 : OOB, L_G + 4096);
      vm += 1;
    }
    __builtin_amdgcn_s_sleep(T2T == 2 ? 27 : 127);
    wf_issue(); wf_check();
    sfor<0, NRE>([&](auto ic) { ldF(decltype(ic)::v); });
    sfor<0, 4>([&](auto ic) { dmaF(NRE + decltype(ic)::v, decltype(ic)::v); });
    sfor<NRE + 4, NRE + 4 + LFE>([&](auto ic) { ldF(decltype(ic)::v); });
#pragma nounroll
    for (int it = 0; it < SPIN_CAP; ++it) {
      unsigned v = ld1c(sa_, 0, PRG + (grp / 3) * (14 * 128));
      got(0, v);
      if (__all(v == tag)) break;
      __builtin_amdgcn_s_sleep(8);
    }
    if (b == 249) {
#pragma nounroll
      for (int it = 0; it < SPIN_CAP; ++it) {
        unsigned v = ld1c(sa_, lane_id() * 128, RDY + wr * 8192);
        got(0, v);
        if (__all(v == tag)) break;
        __builtin_amdgcn_s_sleep(8);
      }
      BAR();
      if (wave == 0) st1l(ep + 1, sa_, CTL);
    }
    ry_issue();
    __builtin_amdgcn_s_sleep(TSL);
  } else {
    constexpr int PITCH = ATT ? 512 : 256, TOFF = ATT ? 256 : 8192;
    unsigned xb[2], wb;
    {
      const int l = lane_id(), r = l & 31, hf = l >> 5, q = (l >> 2) & 3;
#pragma unroll
      for (int k = 0; k < 2; ++k) xb[k] = opq((ROLE * 96 + r) * 128 + (((4 * k + hf) ^ ((r >> 1) & 7)) << 4));
      wb = opq((8 * hf + q) * PITCH + ((wr ^ q) << 6) + ((l >> 4) & 1) * 32 + (l & 1) * 16 + (l & 2) * 4);
    }
    f32x16 acc[3][2];
    f32x4 a4[3][2][4];
    bf16x8 xf[2][2][3], wf[2][2][2];
    f32x4 cs4 = {0, 0, 0, 0};
    unsigned wa, xa;
    auto rd = [&](int s, int ks, int part) {
      const int bf = s & 1, ws_ = wslot(s), xs = xslot(s >> 1);
      const bool wfar = ws_ > 49152, xfar = xs > 40960;
      if (part == 0) {
        if (ks == 0) wa = wfar ? addk(wb, ws_) : wb;
        xa = ks ? xadk(xb[bf], xfar ? xs : 0) : xfar ? addk(xb[bf], xs) : xb[bf];
      }
      const unsigned w0 = wa + (wfar ? 0 : ws_) + 16 * ks * PITCH, x0 = xa + (xfar ? 0 : xs);
      if (part < 2) wf[bf][ks][part] = cat8(lds_tr1(w0 + part * TOFF), lds_tr1(w0 + part * TOFF + 4 * PITCH));
      xf[bf][ks][part] = lds_b128(x0 + part * 4096);
    };
    u32x4 gr[3], cq;
    unsigned csb = CSP + (ATT ? 14 + G : slice) * 8192 + wave * 1024, css = cj < 2 ? csb + (2 * th + cj) * 256 : OOB;
    SPIN(csb); SPIN(css);
    auto gr_load = [&]() {
      const int r = lane_id() & 31;
#pragma unroll
      for (int t = 0; t < 3; ++t) gr[t] = ld4c(sa_, tau_of(3 * ROLE + t, r) * 16, GRN);
    };
    auto h_vo = [&]() {
      const int l = lane_id();
      if (!ATT) return (unsigned)(((l >> 4) & 1) * 1024 + (l >> 5) * 256 + (l & 15) * 16);
      const int tl = (HS ? 8 * ROLE : (3 + W64) * wr) + (l >> 4), t16 = 11 * pn + tl, ft = th * 64 + t16;
      return ((W64 || l < 48) && tl < 11 && t16 < 64) ? (ft >> 2) * GRP + (ft & 3) * 1024 + (l & 15) * 16 : OOB;
    };
    auto h_so = [&](int n) {
      if (ATT && HS) {
        const int oc = 10 * G + 5 * (wr >> 1) + (n >= 2 ? 2 : 3 * (wr & 1) + n);
        return (unsigned)((54 + (oc >> 2)) * 4096 + (oc & 3) * 256 + (n >= 2 ? 8 * (wr & 1) : 0));
      }
      if (ATT) { const int oc = 10 * G + 5 * ROLE + n; return (unsigned)((54 + (oc >> 2)) * 4096 + (oc & 3) * 256); }
      const int T = t32_of(3 * ROLE + (n >> 1)), kb = slice * 4 + wr;
      return (T < 64 && kb < 54) ? (T >> 1) * GRP + kb * 4096 + (T & 1) * 2048 + (n & 1) * 512 : OOB;
    };
    float rs[3], nm[3], cs[2][16];
    int csa;
    const unsigned hvo = opq(h_vo());
    unsigned hso[6], glo = 0, cvo, rso = wr == 0 ? RDY + (ATT ? 160 + job : job) * 128 : OOB;
    u32x4 fil, mk, csd;
    SPIN(rso);
#pragma unroll
    for (int n = 0; n < (ATT && HS ? 3 : 6 - ATT); ++n) { hso[n] = h_so(n); SPIN(hso[n]); }
    {
      const int l = lane_id(), g = l >> 4;
      if (ROLE == 0) glo = opq(tau_of(min(2 * wr + (l >> 5), 5), l & 31) * 16);
      cvo = opq((l & 12) ? OOB : ((l & 1) * 16 + (g & 1) * 8 + (g >> 1) * 4 + (l & 2)) * 8);
      const unsigned m = ((g ^ l) & 1) ? 0u : ONES;
      mk = u32x4{m, m, m, m};
      PIN(mk);
    }
    auto vops = [&](auto sc) {
      constexpr int s = decltype(sc)::v;
      if (s == IPB) ipf(1);
      if (s == IPB2) ipf(2);
      ring_ops(sc);
      if (!ATT) {
        if (s == TL) tu_load(job * 8 + wave, tr_);
        if (s == (TL < -1 ? 0 : TL + TD)) tu_store(job * 8 + wave, tr_, vm - idT);
        if (s == (TL < -1 ? 0 : TL + TD) + 2) tu_flag(job * 8 + wave);
      }
      if (s == TLW) touch();
      if (s == SL) st_load();
      if (SL >= -1 && s == SL + TD) st_take();
      if constexpr (s >= CL0 && s < CL0 + CLS * (6 - ATT) && (s - CL0) % CLS == 0 && !((NOCL >> !ATT) & 1) && !(ATT && AFG)) {
        st4f(fil, sa_, hvo, hso[(s - CL0) / CLS]);
        vm += 1;
      }
      if constexpr (s >= CL0 && s < CL0 + 6 - ATT && ((PTC >> !ATT) & 1)) {
        dmap(sa_, (l16 & 112) ? OOB : hvo, hso[s - CL0], L_G + 4096);
        vm += 1;
      }
      if (s == CL0 + 5 * CLS + 5 && ROLE == 0) { st1l(tag, sa_, rso); vm += 1; }
      if (s == 18 && ROLE == 0 && !ATT) { st1l(tag, sa_, rso + (PRG - RDY)); vm += 1; }
      if (SPF && (s == SPF || s == SPF + 1)) {
        const int l = lane_id();
        dmap(sa_, (s == SPF || l < 29) ? l * 128 : OOB, W2T + ((bi * 8 + wave) * 93 + (s - SPF) * 64) * 128, L_G + 4096);
        vm += 1;
      }
      if (s == SWF - WFD) wf_issue();
      if (s == SWF) wf_check();
      sfor<0, 5 * (NRE - ATT)>([&](auto kc) {
        constexpr int k = decltype(kc)::v;
        if (s == SWF + k * (FSP - SWF) / (5 * (NRE - ATT))) {
          F[k / 5][k % 5] = ld4o(sa_, l16, fso[k / 5], (k % 5) * 1024); vm += 1;
          idF[k / 5] = vm;
        }
      });
      if (s == GL && ROLE == 0) {
        dma1s(sa_, glo, GRN, wr * 1024, L_G);
        vm += 1;
      }
      if (s == CSS) { st4o(csd, sa_, cvo, css); vm += 1; }
      if (s == CSL) { cq = ld4o(sa_, l16, csb, 0); vm += 1; idC = vm; }
      if (s == CST) {
        got(vm - idC, cq);
#pragma nounroll
        for (int it = 0; it < SPIN_CAP && !__all(cq[1] == tag && cq[3] == tag); ++it) { __builtin_amdgcn_s_sleep(2); cq = ld4o(sa_, l16, csb, 0); got(0, cq); }
        f32x2 r = sw16(__uint_as_float(cq[0]), __uint_as_float(cq[2]));
        const float t = r[0] + r[1];
        r = sw32(t, t);
        *(LDS_AS float*)(L_CS + wave * 128 + ((l16 >> 1) & 120) + ((l16 >> 6) & 4)) = r[0] + r[1];
      }
      if (s == 15) dmaF(NRE, 0);
      if (s == 17 && !ATT) dmaF(NRE + 1, 1);
      if (s == 18 && !RYL) ry_issue();
      if (s == 19 && !ATT) { dmaF(NRE + 2, 2); if (!NCS || !ROLE) dmaF(NRE + 3, 3); }
      SB();
    };
    auto sync = [&](int s) {
      if (s < 19) wvl(vm - max(idW[s + 1], idX[(s + 1) >> 1])); else WAIT_L0();
      BAR();
      SB();
    };

    if (IPF == 2 || (IPF == 3 && ROLE == 0)) ipf(0);
    PT(7);
    if (HO & 4) first();
    if ((HO >> ROLE) & 1) rest();
    if (!(HO & 4)) first();
    wv(vm - idX[0]); BAR();
    TS(3);
#pragma unroll
    for (int ks = 0; ks < 2; ++ks)
#pragma unroll
      for (int part = 0; part < 3; ++part) rd(0, ks, part);
    SB();
    vops(IC<-1>{});
    sync(0);
    if (MPR && ROLE) __builtin_amdgcn_s_setprio(1);
    sfor<0, 20>([&](auto sc) {
      constexpr int s = decltype(sc)::v, bf = s & 1;
      if (s == TSA) TS(4);
      if (s == TSA + TSD) TS(5);
      if (s == TSA + 2 * TSD) TS(6);
      if (s == TSA + 3 * TSD) TS(7);
      if (ROLE == 0) vops(sc);
      if (s < 10 && cj == s / 5) {
        cs4 = mfma(wf[bf][0][ROLE], as8(mk), cs4);
        cs4 = mfma(wf[bf][1][ROLE], as8(mk), cs4);
      }
      SB();
#pragma unroll
      for (int i = 0; i < 12; ++i) {
        const int ks = i / 6, t = (i % 6) >> 1, c = (i & 1) ^ (SNK & t & 1);
        mac32(acc[t][c], wf[bf][ks][c], xf[bf][ks][t], s == 0 && ks == 0);
        SB();
        if (s < 19 && i % 6 < 3) { rd(s + 1, ks, i % 6); SB(); }
        if (s == CL0 - 1 + ROLE && i == 4 && !(ATT && AFG)) { fil = u32x4{~0u, ~0u, ~0u, ~0u}; PIN(fil); SB(); }
        if (s == CSS - 1 + ROLE && i == 10) {
          const bool hi = cvo & 16;
          csd = u32x4{__float_as_uint(hi ? cs4[2] : cs4[0]), tag, __float_as_uint(hi ? cs4[3] : cs4[1]), tag};
          PIN(csd); SB();
        }
        if (NCS && s == 19 && i >= 1 && i < 5) {
          if (i == 1) csa = opq(L_CS + wr * 128 + ((l16 >> 4) & 32));
#pragma unroll
          for (int k = 2 * i - 2; k < 2 * i; ++k) {
            const f32x4 v = lds_f4(csa + (k >> 2) * 512 + (k & 2) * 32 + (k & 1) * 16);
#pragma unroll
            for (int j = 0; j < 4; ++j) cs[k >> 2][(k & 3) * 4 + j] = v[j];
          }
          SB();
        }
        if (s == 19 && i == 9) {
          const unsigned ga = opq(L_G + ROLE * 1536 + (l16 & 511));
#pragma unroll
          for (int t = 0; t < 3; ++t) gr[t] = lds_u4(ga + t * 512);
          SB();
        }
        if (ROLE == 0 && s < 19 && i == 11 - BP0) sync(s + 1);
      }
      if (ROLE == 1) { vops(sc); if (s < 19) sync(s + 1); }
    });
    if (MPR == 1 && ROLE) __builtin_amdgcn_s_setprio(0);
    TS(8);
    {
      auto ok = [&]() { return __all(gr[0][1] == tag && gr[0][3] == tag && gr[1][1] == tag && gr[1][3] == tag && gr[2][1] == tag && gr[2][3] == tag); };
#pragma nounroll
      for (int it = 0; it < SPIN_CAP && !ok(); ++it) { __builtin_amdgcn_s_sleep(2); gr_load(); gotk(0, gr); }
#pragma unroll
      for (int t = 0; t < 3; ++t) { rs[t] = __uint_as_float(gr[t][0]); nm[t] = __uint_as_float(gr[t][2]); PIN(rs[t]); PIN(nm[t]); }
    }
    asm volatile("s_nop 15\n\ts_nop 3" ::: "memory");
#pragma unroll
    for (int t = 0; t < 3; ++t) {
      PIN(acc[t][0]); PIN(acc[t][1]);
    }
    if constexpr (!ATT) {
      ET(3);
      if (RYL < 0) { ry_issue(); SB(); }
#pragma unroll
      for (int t = 0; t < 3; ++t) {
        const float a2 = rs[t] * rs[t], k1 = rs[t] * -2.3022082f, k3 = a2 * rs[t] * -0.10294324f, ia2 = __builtin_amdgcn_rcpf(a2);
#pragma unroll
        for (int o = 0; o < 2; ++o) {
          u32x4 w;
#pragma unroll
          for (int q = 0; q < 4; ++q) {
            const int r = 8 * o + 2 * q;
            f32x2 g = fma2(sp2(nm[t]), f32x2{cs[0][r], cs[0][r + 1]}, f32x2{acc[t][0][r], acc[t][0][r + 1]});
            f32x2 u = fma2(sp2(nm[t]), f32x2{cs[1][r], cs[1][r + 1]}, f32x2{acc[t][1][r], acc[t][1][r + 1]});
            f32x2 e = fma2(g * g, sp2(k3), sp2(k1)) * g;
            e = f32x2{__builtin_amdgcn_exp2f(e[0]), __builtin_amdgcn_exp2f(e[1])};
            e = fma2(e, sp2(ia2), sp2(ia2));
            e = f32x2{__builtin_amdgcn_rcpf(e[0]), __builtin_amdgcn_rcpf(e[1])};
            w[q] = nofill(pk2(g * u * e));
          }
          st4h(w, sa_, hvo, hso[2 * t + o]);
          vm += 1;
          SB();
        }
        if (t < 2) ET(4 + 2 * t);
        if (t < LFE) ldF(NRE + 4 + t);
        if (RYL && t == RYL - 1) ry_issue();
        if (t == (ROLE ? EMT1 : EMT0)) early();
        if (t < 2) ET(5 + 2 * t);
        SB();
      }
      TS(9);
    } else {
      constexpr float QS = 0.22360679775f * 1.44269504089f;
      ET(3);
      if (RYL && RYA == 0) { ry_issue(); SB(); }
      {
        const int l = lane_id();
        const unsigned qa = opq(L_Q + (ROLE * 96 + (l & 31)) * KP + (wr * 4 + (l >> 5)) * 16);
        const int bp = wr ? VP : KP;
        unsigned qb = (wr ? L_V + (wr - 1) * 64 : L_Q + 256) + (ROLE * 96 + (l & 31)) * bp + (l >> 5) * 16;
        const float f0 = wr < 3 ? QS : 1.0f, f1 = wr < 2 ? QS : 1.0f;
#pragma unroll
        for (int t = 0; t < 3; ++t) {
#pragma unroll
          for (int c = 0; c < 2; ++c)
#pragma unroll
            for (int o = 0; o < 2; ++o) {
              const float a = c ? rs[t] : rs[t] * (o ? f1 : f0);
              u32x4 w;
#pragma unroll
              for (int q = 0; q < 4; ++q) {
                const int r = 8 * o + 2 * q;
                w[q] = pk2(fma2(sp2(nm[t]), f32x2{cs[c][r], cs[c][r + 1]}, f32x2{acc[t][c][r], acc[t][c][r + 1]}) * sp2(a));
              }
              if (c == 0 || o == 0 || wr < 3) *(LDS_AS u32x4*)((DQ && c ? qb : qa + t * 32 * KP + c * 256) + o * 32) = w;
              if (DQ && c && o && t < 2) qb = opq(qb + 32 * bp);
            }
          SB();
          if (t < 2) ldF(t ? NRE + 1 : NRE - 1);
          if (AXF && AXP == 1 && t == 2) ldF(NRE + 3);
          SB();
        }
      }
      ET(4);
      WAIT_L0(); BAR();
      ET(5);
      if (RYL && RYA == 1) { ry_issue(); SB(); }
      const int l = lane_id(), j = l & 3;
      constexpr int NU = 2;
      const unsigned ra = opq(L_Q + (8 + (48 + 16 * W64) * wr + l) * KP + 80 * ROLE);
      const unsigned va = opq(L_V + (8 + (48 + 16 * W64) * wr + (l & 48) + j * 4 + ((l >> 2) & 3)) * VP + 80 * ROLE);
      u32x4 Q[5], K[3][5];
      if (AQO) {
#pragma unroll
        for (int i = 0; i < 5; ++i) {
          Q[i] = lds_u4(ra + 8 * KP + i * 16);
#pragma unroll
          for (int m = 0; m < 3; ++m) K[m][i] = lds_u4(ra + (8 - 4 * m) * KP + 160 + i * 16);
          SB();
        }
      } else if (W64 ? wr < 3 : (!L48 || l < 48)) {
#pragma unroll
        for (int i = 0; i < 5; ++i) Q[i] = lds_u4(ra + 8 * KP + i * 16);
#pragma unroll
        for (int m = 0; m < 3; ++m)
#pragma unroll
          for (int i = 0; i < 5; ++i) K[m][i] = lds_u4(ra + (8 - 4 * m) * KP + 160 + i * 16);
      }
      SB();
      if (ARE == 1 && ROLE) dmaF(NRE + 5, 3);
      ldF(NRE + 2);
      if (ARE == 2 && ROLE) dmaF(NRE + 5, 3);
      if (AXF && AXP == 0) ldF(NRE + 3);
      SB();
      auto half = [&](const u32x4 (&v)[5], int i) { return u32x2{v[i >> 1][(i & 1) * 2], v[i >> 1][(i & 1) * 2 + 1]}; };
      f32x4 lg[2][3];
      u32x2 V[3][10];
      auto rdV = [&](int i) {
#pragma unroll
        for (int m = 0; m < 3; ++m) V[m][i] = __builtin_bit_cast(u32x2, lds_tr1(va + (8 - 4 * m) * VP + i * 8));
      };
#pragma unroll
      for (int i = 0; i < 10 * !!AQO; ++i) {
#pragma unroll
        for (int m = 0; m < 3; ++m) lg[i / 5][m] = mfma4(half(K[m], i), half(Q, i), i % 5 == 0 ? f32x4{0, 0, 0, 0} : lg[i / 5][m]);
        SB();
        if (AQO == 1 && (i & 1)) { rdV(i - 1); rdV(i); SB(); }
      }
#pragma unroll
      for (int c = 0; c < 5 * !AQO; ++c)
#pragma unroll
        for (int h = 0; h < 2; ++h)
#pragma unroll
          for (int m = 0; m < 3; ++m) lg[h][m] = mfma4(half(K[m], 5 * h + c), half(Q, 5 * h + c), c == 0 ? f32x4{0, 0, 0, 0} : lg[h][m]);
      SB();
      if (AQO != 1 && (W64 ? wr < 3 : (L48 < 2 || l < 48))) {
#pragma unroll
        for (int i = 0; i < 10; ++i)
#pragma unroll
          for (int m = 0; m < 3; ++m) V[m][i] = __builtin_bit_cast(u32x2, lds_tr1(va + (8 - 4 * m) * VP + i * 8));
      }
      if (AXF && AXP == 2) ldF(NRE + 3);
      if (AXF == 2) ldF(NRE + 4);
      if (ARE == 3 && ROLE) dmaF(NRE + 5, 3);
      if (DQ) { SB(); BAR(); }
      if (DQ == 1) { dmaF(NRE + 4, 1); if (!ROLE) dmaF(NRE + 5, 2); }
      if (DQ == 3) dmaF(NRE + 4, 1);
      SB();
      u32x2 Pm[2][3];
      const unsigned m0 = j == 0 ? 0xffffu : ~0u, m1 = j == 3 ? ~0u : j == 2 ? 0xffffu : 0u;
#pragma unroll
      for (int h = 0; h < NU; ++h) {
        if (pn == 0 && (HS ? !ROLE && !h : wr == 0)) {
          constexpr float NEG = -1e9f * 1.44269504089f;
#pragma unroll
          for (int i = 0; i < 4; ++i) { if (l < 4) lg[h][1][i] = NEG; if (l < 8) lg[h][2][i] = NEG; }
        }
        float s[8];
        s[0] = lg[h][0][0];
#pragma unroll
        for (int i = 1; i < 4; ++i) s[i] = i <= j ? lg[h][0][i] : lg[h][2][i];
#pragma unroll
        for (int i = 0; i < 4; ++i) s[4 + i] = lg[h][1][i];
        const float mx = fmaxf(fmaxf(fmaxf(s[0], s[1]), fmaxf(s[2], s[3])), fmaxf(fmaxf(s[4], s[5]), fmaxf(s[6], s[7])));
#pragma unroll
        for (int i = 0; i < 8; ++i) s[i] = __builtin_amdgcn_exp2f(s[i] - mx);
        const float inv = __builtin_amdgcn_rcpf(((s[0] + s[1]) + (s[2] + s[3])) + ((s[4] + s[5]) + (s[6] + s[7])));
        const unsigned a0 = pk(s[0] * inv, s[1] * inv), a1 = pk(s[2] * inv, s[3] * inv);
        Pm[h][0] = u32x2{a0 & m0, a1 & m1};
        Pm[h][1] = u32x2{pk(s[4] * inv, s[5] * inv), pk(s[6] * inv, s[7] * inv)};
        Pm[h][2] = u32x2{a0 & ~m0, a1 & ~m1};
      }
      ET(6);
      if (RYL && RYA == 2) { ry_issue(); SB(); }
      if (AB2) { WAIT_L0(); BAR(); }
      ET(7);
      if (EAP == 0) early();
      if (DQ == 2) dmaF(NRE + 4, 1);
      if (DQ >= 2 && !ROLE) { dmaF(NRE + 5, 2); SB(); }
      if (ADM == 1) { dmaF(NRE + 3, 1); dmaF(NRE + 4, 2); dmaF(NRE + 5, 3); SB(); }
      if ((ADM == 3 || ADM == 4) && (AXF < 2 || ADI < 0)) { dmaF(NRE + 3 + AXF, 1 + AXF); SB(); }
      if (ADM == 4) { dmaF(NRE + 4, 2); SB(); }
      f32x4 ov[10];
#pragma unroll
      for (int m = 0; m < 3; ++m)
#pragma unroll
        for (int i = 0; i < 5 * NU; ++i) ov[i] = mfma4(V[m][i], Pm[i / 5][m], m == 0 ? f32x4{0, 0, 0, 0} : ov[i]);
#pragma unroll
      for (int n = 0; n < 3 * NU * !!HS; ++n) {
        const int q = n / 3 * 5 + n % 3 * 2;
        const unsigned so = hso[n % 3] + n / 3 * GRP, w0 = pk(ov[q][0], ov[q][1]), w1 = pk(ov[q][2], ov[q][3]);
        if (n % 3 == 2) st2h(u32x2{w0, w1}, sa_, hvo, so);
        else st4h(u32x4{w0, w1, pk(ov[q + 1][0], ov[q + 1][1]), pk(ov[q + 1][2], ov[q + 1][3])}, sa_, hvo, so);
        vm += 1; idH = vm;
      }
#pragma unroll
      for (int n = 0; n < 5 * !HS; ++n) {
        auto nf = [](unsigned v) { return AFG ? v : nofill(v); };
        st4h(u32x4{nf(pk(ov[2 * n][0], ov[2 * n][1])), nf(pk(ov[2 * n][2], ov[2 * n][3])),
                   nf(pk(ov[2 * n + 1][0], ov[2 * n + 1][1])), nf(pk(ov[2 * n + 1][2], ov[2 * n + 1][3]))}, sa_, hvo, hso[n]);
        vm += 1; idH = vm;
      }
      SB();
      if (EAP == 1) early();
      if (ADM == 2) { dmaF(NRE + 3, 1); dmaF(NRE + 4, 2); dmaF(NRE + 5, 3); SB(); }
      if (ADM == 3 && AXF < 2 && (!AXF || ADI < 0)) { dmaF(NRE + 4 + AXF, 2 + AXF); SB(); }
      TS(9);
    }
  }

  f32x4 o[4][5];
  unsigned fm[2];
  auto a_max = [&](int i, int j) {
    auto w = [&](int k) { return A[i][k >> 2][k & 3]; };
    if (j == 0) { fm[0] = max(max(w(0), w(1)), w(2)); fm[1] = max(max(w(3), w(4)), w(5)); }
    else if (j < 3) { fm[0] = max(max(fm[0], w(4 * j + 2)), w(4 * j + 3)); fm[1] = max(max(fm[1], w(4 * j + 4)), w(4 * j + 5)); }
    else fm[0] = max(max(fm[0], w(14)), w(15));
    PIN(fm[0]); PIN(fm[1]);
  };
  auto a_fix = [&](int i) {
    if (__builtin_expect(__any(max(fm[0], fm[1]) == ~0u), 0)) {
      int it = 0;
      bool bad;
#pragma nounroll
      do {
        __builtin_amdgcn_s_sleep(PSL);
        ldk(A[i], sa_, l16, a_so(i));
        gotk(0, A[i]);
        unsigned m = 0;
#pragma unroll
        for (int k = 0; k < 16; ++k) m = max(m, A[i][k >> 2][k & 3]);
        bad = __any(m == ~0u);
      } while (bad && ++it < SPIN_CAP);
      asm volatile("s_nop 1" ::: "memory");
    }
    SB();
  };
  if (TYPE == 0 || (TYPE == 1 && !ANB)) BAR();
  if (MPR == 2 && ROLE) __builtin_amdgcn_s_setprio(0);
  if (EA == 0) ry_check();
  TS(10);
  unsigned pfv = 0;
  sfor<EA, a_upto(ATT, NS, 0)>([&](auto ic) {
    if (PRF && ROLE && decltype(ic)::v == 0) __builtin_amdgcn_s_setprio(2);
    ldA(decltype(ic)::v);
    if (PRF && ROLE && decltype(ic)::v == 0) __builtin_amdgcn_s_setprio(0);
    if (HPF && decltype(ic)::v == HPF - 1) {
      pfv = ld1c(sa_, l16 < 37 * 16 ? l16 * 8 : OOB, grp * GRP + (ct * 8 + wave) * (37 * 128));
      vm += 1;
    }
  });
  u32x4 xr, xs; u32x2 xr2; unsigned xr1;
  unsigned eo, eo2, fa, ra_[2];
  auto put = [&](int k) { PIN(o[k & 3][k >> 2]); *(LDS_AS f32x4*)(ra_[(k >> 1) & 1] + (k & 1) * PIECE + (k >> 2) * 1024) = o[k & 3][k >> 2]; SB(); };
  auto af_set = [&]() { wv(vm - idH); st1l(tag, sa_, AFL + job * 128 + wave * 4); vm += 1; SB(); };
  unsigned afo, afv;
  auto af_issue = [&]() {
    const int l = lane_id(), t16 = 4 * (grp & 15) + (l & 3), q = t16 / 11, c0 = 32 * ROLE + (8 + 4 * ROLE) * wr;
    const int hu = min(c0 / 5 + ((l >> 2) & 3), (c0 + 7 + 4 * ROLE) / 5);
    if (!HS) afo = opq(((grp >> 4) * 6 + q) * 1024 + (hu >> 1) * 128 + (hu & 1) * 16 + (t16 - 11 * q) / (3 + W64) * 4);
    if (HS) {
      const int hd = min(2 * c0 / 5 + ((l >> 2) & 7), (8 * c0 + 63 + 32 * ROLE) / 20);
      afo = opq(((grp >> 4) * 6 + q) * 1024 + (hd >> 2) * 128 + (hd & 3) * 4 + ((t16 - 11 * q) >> 3) * 16);
    }
    afv = ld1c(sa_, afo, AFL); vm += 1; idG = vm;
  };
  auto af_check = [&]() {
    got(vm - idG, afv);
#pragma nounroll
    for (int it = 0; it < SPIN_CAP && !__all(afv == tag); ++it) {
      __builtin_amdgcn_s_sleep(2);
      afv = ld1c(sa_, afo, AFL);
      got(0, afv);
    }
    SB();
  };
  auto chk = [&](int i) { return !AFG || i < NA || i == OP; };
  if (ATT && AFG && AFE == 1) af_set();
  gotk(vm - idA[0], A[0]); gotk(vm - idF[0], F[0]);
  if (ATT && AFG && !AFE) af_set();
#pragma unroll
  for (int j = 0; j < 4; ++j) a_max(0, j);
  a_fix(0);
  TS(11);
  sfor<0, NS>([&](auto ic) {
    constexpr int i = decltype(ic)::v, pq_ = i + 1 < NS ? piece(ATT, i + 1) : -1, pn_ = (ATT && DQ && pq_ > 1) ? pq_ - 1 + (pq_ == 3 && ROLE) : pq_, a0 = a_upto(ATT, NS, i ? i - 1 : 0), a1 = a_upto(ATT, NS, i);
    if (ROLE && i == PRS) __builtin_amdgcn_s_setprio(1);
    if (i == 9 && !IWR) {
      if (wr < 2) {
#pragma unroll
        for (int k = 0; k < 20; ++k) mac16(o[k & 3][k >> 2], F[i][k >> 2], A[i][k & 3], false);
      }
      SB();
    } else sfor<0, 20>([&](auto kc) {
      constexpr int k = decltype(kc)::v;
      mac16(o[k & 3][k >> 2], F[i][k >> 2], A[i][k & 3], i == 0);
      SB();
      if constexpr (AFG && k == 0 && a1 <= NA && a_upto(ATT, NS, i + 1) > NA) af_issue();
      if constexpr (AFG && k % 3 == 0 && a0 + k / 3 == NA && NA < a1) af_check();
      if constexpr (k % 3 == 0 && a0 + k / 3 < a1) ldA(a0 + k / 3);
      if constexpr (ATT && AFG && AFE >= 2 && i == AFE - 1 && k == 7) af_set();
      if constexpr (IWR && i == NS - 1) {
        if (k == 0) { ra_[0] = opq(l16 + pdst); ra_[1] = opq(l16 + pdst + 2 * PIECE); }
        if (k >= IWR) put(k - IWR);
      }
      if (k == 9) {
        if (ATT && ADM == 0 && i < 3) dmaF(NRE + 3 + i, 1 + i);
        if (ATT && ADM >= 3 && i == ADI + (ADM == 5) && !(ARE && ROLE) && !DQ) dmaF(NRE + 5, 3);
        if (ATT && ANB == 2 && i == ADI) BAR();
        if (ATT && ADM == 5 && i == ADI && !DQ) dmaF(NRE + 4, 2);
        if (NCS && TYPE == 0 && ROLE && i == 0) dmaF(NRE + 3, 3);
        sfor<i + 1, NS>([&](auto jc) { if (i && f_when(ATT, decltype(jc)::v) == i) ldF(decltype(jc)::v); });
        if (PR2 && i == 6 && !ROLE) {
          const int l = lane_id(), g = l >> 4, tau = grp * 64 + wr * 16 + (l & 15), tok = 2 * (tau & 1023) + (tau >> 10);
          eo = (tok * D_ + ct * 80 + g * 8) * 2; eo2 = eo - g * 8;
          asm volatile("buffer_load_dwordx4 %0, %3, %5, 0 offen offset:2048\n\tbuffer_load_dwordx4 %1, %3, %5, 0 offen offset:2112\n\t"
                       "buffer_load_dwordx2 %2, %4, %5, 0 offen offset:2176"
                       : "=&v"(xr), "=&v"(xs), "=&v"(xr2) : "v"(eo), "v"(eo2), "s"(sx) : "memory");
          vm += 3;
        }
        if (!PR2 && i == 6) {
          const int l = lane_id(), g = l >> 4, tau = grp * 64 + (wave >> 1) * 16 + (l & 15), tok = 2 * (tau & 1023) + (tau >> 10);
          eo = (tok * D_ + ct * 80 + (wave & 1) * 32 + g * 8) * 2;
          eo2 = (tok * D_ + ct * 80 + 64 + (wave & 1) * 8 + g * 2) * 2;
          asm volatile("buffer_load_dwordx4 %0, %2, %4, 0 offen offset:2048\n\tbuffer_load_dword %1, %3, %4, 0 offen offset:2048"
                       : "=&v"(xr), "=&v"(xr1) : "v"(eo), "v"(eo2), "s"(sx) : "memory");
          vm += 2;
        }
      }
      if constexpr (pn_ >= 0 && (k & 3) == 3) {
        if (k == 3) { wv(vm - idF[i + 1]); fa = opq(l16 + pdst + pn_ * PIECE); }
        F[i + 1][k >> 2] = lds_u4(fa + (k >> 2) * 1024);
      }
      if (PR2 && ROLE && 20 * i + k == 20 * (NS - 2) + PR2) BAR();
      if constexpr (i + 1 < NS) {
        if (k == CKA) { gotk(vm - idA[i + 1], A[i + 1]); if (pn_ < 0) gotk(vm - idF[i + 1], F[i + 1]); }
        if (k >= CKA && k < CKA + 4 && chk(i + 1)) a_max(i + 1, k - CKA);
      }
      SB();
    });
    if constexpr (i + 1 < NS) { if (chk(i + 1)) a_fix(i + 1); }
    if (HPF && i == 3) pfv = opq(pfv);
  });
  TS(12);
  if (IWR) {
#pragma unroll
    for (int k = 20 - IWR; k < 20; ++k) put(k);
  } else asm volatile("s_nop 15\n\ts_nop 3" ::: "memory");
#pragma unroll
  for (int t = 0; t < 4 * !IWR; ++t) {
    const unsigned a = opq(l16 + pdst + t * PIECE);
#pragma unroll
    for (int n = 0; n < 5; ++n) { PIN(o[t][n]); *(LDS_AS f32x4*)(a + n * 1024) = o[t][n]; }
  }
  {
    const int l = lane_id(), g = l >> 4, opr = wave & 1;
    const unsigned a1 = opq((wave >> 1) * PIECE + (opr * 2 + (g >> 1)) * 1024 + (g & 1) * 256 + (l & 15) * 16);
    const unsigned a2 = opq((wave >> 1) * PIECE + 4096 + opr * 256 + (l & 15) * 16 + g * 10240);
    char* o1 = (char*)p.out + eo; char* o2 = (char*)p.out + eo2;
    got(0, xr, xr1);
    f32x4 xe = {blo(xr[0]), blo(xr[1]), blo(xr[2]), blo(xr[3])}, xd = {bhi(xr[0]), bhi(xr[1]), bhi(xr[2]), bhi(xr[3])};
    PIN(xe); PIN(xd);
    SB();
    WAIT_L0(); BAR();
    asm volatile(".Lk_end%0:" :: "i"(TYPE * 2 + ROLE));
    TS(13);
    f32x4 t[4], e[8], d[8];
#pragma unroll
    for (int q = 0; q < 4; ++q) t[q] = lds_f4(a2 + (q & 1) * 5120 + (q >> 1) * 512);
#pragma unroll
    for (int v = 0; v < 8; ++v) { e[v] = lds_f4(a1 + v * 5120); d[v] = lds_f4(a1 + v * 5120 + 512); }
    SB();
    auto fold = [](f32x4 t) {
      const f32x2 a = sw32(t[0], t[2]), b = sw32(t[1], t[3]), r = sw16(a[0] + a[1], b[0] + b[1]);
      return r[0] + r[1];
    };
    const float e5 = fold(t[0] + t[1]), d5 = fold(t[2] + t[3]);
    *(unsigned*)o2 = pk(e5 + blo(xr1), d5 + bhi(xr1));
    SB();
#pragma unroll
    for (int v = 0; v < 8; ++v) { xe += e[v]; xd += d[v]; }
    u32x4 r;
#pragma unroll
    for (int q = 0; q < 4; ++q) r[q] = pk(xe[q], xd[q]);
    *(u32x4*)o1 = r;
  }
  TS(14); TSR(15);
  TSOUT();
}

__global__ __launch_bounds__(512, 2) void k_fused(P p) {
  const int wave = RFL(threadIdx.x >> 6);
  i32x4 sc0;
  {
    uint64_t pc; unsigned len;
    asm volatile("s_getpc_b64 %0\n.Lk_here:\n\ts_mov_b32 %1, .Lk_end0-.Lk_here\n\ts_max_i32 %1, %1, .Lk_end1-.Lk_here\n\ts_max_i32 %1, %1, .Lk_end2-.Lk_here\n\t"
                 "s_max_i32 %1, %1, .Lk_end3-.Lk_here\n\ts_max_i32 %1, %1, .Lk_end4-.Lk_here\n\ts_max_i32 %1, %1, .Lk_end5-.Lk_here" : "=s"(pc), "=s"(len) :: "scc");
    sc0 = mk_srd((const void*)pc, len & ~3u);
  }
  const int b = blockIdx.x, xcd = b & 7, i = b >> 3, th = xcd >> 2, wq = xcd & 3;
  if (i < 12) {
    int job = (th * 6 + (i >> 1)) * 8 + wq * 2 + (i & 1);
    if (wave < 4) body<1, 0>(p, job, wave, sc0); else body<1, 1>(p, job, wave, sc0);
  } else if (i - 12 < (wq == 0 ? 20 : 19)) {
    int n = wq * 19 + min(wq, 1) + (i - 12), sl, pn;
    if (th == 0) { if (n < 42) { sl = n / 6; pn = n - sl * 6; } else { int m = n - 42; sl = m / 5; pn = m - sl * 5; sl += 7; } }
    else { if (n < 35) { sl = n / 5; pn = 6 + n - sl * 5; } else { int m = n - 35; sl = m / 6; pn = 5 + m - sl * 6; sl += 7; } }
    if (wave < 4) body<0, 0>(p, pn * 14 + sl, wave, sc0); else body<0, 1>(p, pn * 14 + sl, wave, sc0);
  } else {
    const int sp = th * 3 + wq - 1;
    if (wave < 4) body<2, 0>(p, sp, wave, sc0); else body<2, 1>(p, sp, wave, sc0);
  }
}

extern "C" void kernel_launch(void* const* d_in, const int* in_sizes, int n_in, void* d_out, int out_size, void* d_ws,
                              size_t ws_size, hipStream_t stream) {
  P p;
  p.x = d_in[0]; p.wqkv = d_in[1]; p.wo = d_in[2]; p.wg = d_in[4]; p.wu = d_in[5]; p.wd = d_in[6];
  p.out = d_out; p.ws = (char*)d_ws;
  (void)hipFuncSetAttribute((const void*)k_fused, hipFuncAttributeMaxDynamicSharedMemorySize, LDS_BYTES);
  k_fused<<<256, 512, LDS_BYTES, stream>>>(p);
}
```
